```python
import torch

def _softmax(x, axis=-1):
    return torch.softmax(x, dim=axis)

def _gelu(x):
    return torch.nn.functional.gelu(x, approximate='tanh')

def _silu(x):
    return torch.nn.functional.silu(x)

def _sigmoid(x):
    return torch.sigmoid(x)

def _layernorm(x, axis=-1, eps=1e-05):
    m = x.mean(dim=axis, keepdim=True)
    v = x.var(dim=axis, keepdim=True, unbiased=False)
    return (x - m) / torch.sqrt(v + eps)

def _scatter_add(tgt, idx, src, axis=0):
    return tgt.clone().index_add_(axis, idx, src)

def _segment_sum(src, seg, n):
    out = torch.zeros((n,) + src.shape[1:], dtype=src.dtype)
    return out.index_add_(0, seg, src)

def _pad(x, pw):
    flat = tuple((v for pair in reversed(pw) for v in pair))
    return torch.nn.functional.pad(x, flat)
kernel_name = 'interleave_layers__grouped_kv_mqa__memory_aug_recurrent_mem_tr__prompt'

def setup_inputs():

    _w = lambda a: (lambda t: t.to(torch.bfloat16) if t.is_floating_point() else t)(torch.as_tensor(a))

    def _A_setup():
        B, S, D, H = (1, 2048, 512, 32)
        Dh = D // H
        return {'x': _w((lambda *s: torch.randn(*s, dtype=torch.bfloat16) * 0.05)(B, S, D)), 'wq': _w((lambda *s: torch.randn(*s, dtype=torch.bfloat16) * 0.05)(D, H * Dh)), 'wk': _w((lambda *s: torch.randn(*s, dtype=torch.bfloat16) * 0.05)(D, Dh)), 'wv': _w((lambda *s: torch.randn(*s, dtype=torch.bfloat16) * 0.05)(D, Dh)), 'wo': _w((lambda *s: torch.randn(*s, dtype=torch.bfloat16) * 0.05)(H * Dh, D))}
    _A_in = _A_setup()

    def _B_setup():
        B, S, Dm = (1, 2048, 512)
        H, Dh = (32, 128)
        M, seg = (8, 32)
        seg = min(seg, S)
        S = S // seg * seg
        return {'x': _w((lambda *s: torch.randn(*s, dtype=torch.bfloat16) * 0.05)(B, S, Dm)), 'mem0': _w((lambda *s: torch.randn(*s, dtype=torch.bfloat16) * 0.8)(B, M, Dm)), 'Wq': _w((lambda *s: torch.randn(*s, dtype=torch.bfloat16) * 1.6)(Dm, H, Dh) * Dm ** (-0.5)), 'Wk': _w((lambda *s: torch.randn(*s, dtype=torch.bfloat16) * 1.6)(Dm, H, Dh) * Dm ** (-0.5)), 'Wv': _w((lambda *s: torch.randn(*s, dtype=torch.bfloat16) * 0.8)(Dm, H, Dh) * Dm ** (-0.5)), 'Wo': _w((lambda *s: torch.randn(*s, dtype=torch.bfloat16) * 0.8)(H, Dh, Dm) * (H * Dh) ** (-0.5)), 'seg_len': seg}
    _B_in = _B_setup()
    return {'A_x': _A_in['x'], 'A_wq': _A_in['wq'], 'A_wk': _A_in['wk'], 'A_wv': _A_in['wv'], 'A_wo': _A_in['wo'], 'B_x': _B_in['x'], 'B_mem0': _B_in['mem0'], 'B_Wq': _B_in['Wq'], 'B_Wk': _B_in['Wk'], 'B_Wv': _B_in['Wv'], 'B_Wo': _B_in['Wo'], 'B_seg_len': _B_in['seg_len']}

def reference(A_x, A_wq, A_wk, A_wv, A_wo, B_x, B_mem0, B_Wq, B_Wk, B_Wv, B_Wo, B_seg_len):

    def _A_ref(x, wq, wk, wv, wo):
        B, S, D = x.shape
        H = 32
        Dh = D // H
        q = (lambda x, *d: x.permute(*d))((x @ wq).reshape(B, S, H, Dh), 0, 2, 1, 3)
        k = x @ wk
        v = x @ wv
        scores = torch.einsum('bhqd,bkd->bhqk', q, k) * Dh ** (-0.5)
        p = _softmax(scores, axis=-1)
        o = torch.einsum('bhqk,bkd->bhqd', p, v)
        return (lambda x, *d: x.permute(*d))(o, 0, 2, 1, 3).reshape(B, S, H * Dh) @ wo

    def _B_ref(x, mem0, Wq, Wk, Wv, Wo, seg_len):
        B, S, Dm = x.shape
        H, Dh = (Wq.shape[1], Wq.shape[2])
        M = mem0.shape[1]
        scale = Dh ** (-0.5)
        n_seg = S // seg_len
        L = M + seg_len
        idx = torch.arange(L)
        mask = (idx[:, None] < M) | (idx[:, None] >= idx[None, :])
        mem = mem0
        outs = []
        for s in range(n_seg):
            xs = x[:, s * seg_len:(s + 1) * seg_len, :].clone()
            h = torch.concatenate([mem, xs], axis=1)
            q = torch.einsum('btd,dhe->bhte', h, Wq)
            k = torch.einsum('btd,dhe->bhte', h, Wk)
            v = torch.einsum('btd,dhe->bhte', h, Wv)
            scores = torch.einsum('bhqe,bhke->bhqk', q, k) * scale
            scores = torch.where(mask[None, None], scores, -1000000000.0)
            p = _softmax(scores, axis=-1)
            o = torch.einsum('bhqk,bhke->bhqe', p, v)
            o = torch.einsum('bhte,hed->btd', o, Wo)
            mem = o[:, :M, :].clone()
            outs.append(o[:, M:, :])
        return torch.concatenate(outs, axis=1)
    x = A_x
    x = x + _A_ref(_layernorm(x, axis=-1), A_wq, A_wk, A_wv, A_wo)
    x = x + _B_ref(_layernorm(x, axis=-1), B_mem0, B_Wq, B_Wk, B_Wv, B_Wo, B_seg_len)
    x = x + _A_ref(_layernorm(x, axis=-1), A_wq, A_wk, A_wv, A_wo)
    x = x + _B_ref(_layernorm(x, axis=-1), B_mem0, B_Wq, B_Wk, B_Wv, B_Wo, B_seg_len)
    return x
```

```cpp
#include <hip/hip_runtime.h>
#include <hip/hip_bf16.h>
#include <stdint.h>

typedef unsigned short u16;
using bf16x8 = __attribute__((ext_vector_type(8))) short;
using f32x4  = __attribute__((ext_vector_type(4))) float;
using f32x16 = __attribute__((ext_vector_type(16))) float;
using u32x4  = __attribute__((ext_vector_type(4))) unsigned;
using u32x2  = __attribute__((ext_vector_type(2))) unsigned;
#define DI __device__ __forceinline__

DI float bf2f(u16 x) { return __uint_as_float(((unsigned)x) << 16); }
DI u16 f2bf(float f) { __bf16 b = (__bf16)f; return __builtin_bit_cast(u16, b); }
DI float rbf(float f) { return bf2f(f2bf(f)); }
DI unsigned pack2(float lo, float hi) { return (unsigned)f2bf(lo) | ((unsigned)f2bf(hi) << 16); }

constexpr int S_ = 2048, D_ = 512;
constexpr int AH = 32, ADH = 16;
constexpr int BH = 32, BDH = 128, BM_ = 8, SEG = 32, NSEG = 64;
constexpr int BN3 = 3 * BH * BDH;
constexpr int BHD = BH * BDH;

__global__ __launch_bounds__(256) void ln_kernel(const u16* __restrict__ x, u16* __restrict__ y) {
  const int row = blockIdx.x * 4 + (threadIdx.x >> 6), lane = threadIdx.x & 63;
  const u32x4 v = *(const u32x4*)(x + (size_t)row * D_ + lane * 8);
  float f[8];
#pragma unroll
  for (int i = 0; i < 4; ++i) { f[2 * i] = __uint_as_float(v[i] << 16); f[2 * i + 1] = __uint_as_float(v[i] & 0xffff0000u); }
  float s = 0.f;
#pragma unroll
  for (int i = 0; i < 8; ++i) s += f[i];
#pragma unroll
  for (int o = 32; o >= 1; o >>= 1) s += __shfl_xor(s, o);
  const float mean = s * (1.0f / D_);
  float q = 0.f;
#pragma unroll
  for (int i = 0; i < 8; ++i) { const float d = f[i] - mean; q += d * d; }
#pragma unroll
  for (int o = 32; o >= 1; o >>= 1) q += __shfl_xor(q, o);
  const float var = q * (1.0f / D_);
  const float mb = rbf(mean), vb = rbf(var);
  const float den = rbf(sqrtf(rbf(vb + 1e-5f)));
  u32x4 o;
#pragma unroll
  for (int i = 0; i < 4; ++i) o[i] = pack2(rbf(f[2 * i] - mb) / den, rbf(f[2 * i + 1] - mb) / den);
  *(u32x4*)(y + (size_t)row * D_ + lane * 8) = o;
}

__global__ __launch_bounds__(256) void transpose_kernel(const u16* __restrict__ in, u16* __restrict__ out, int K, int N) {
  __shared__ u16 t[64][66];
  const int k0 = blockIdx.y * 64, n0 = blockIdx.x * 64;
  const int tx = threadIdx.x & 63, ty = threadIdx.x >> 6;
  for (int r = ty; r < 64; r += 4) { const int n = n0 + tx; t[r][tx] = (n < N) ? in[(size_t)(k0 + r) * N + n] : (u16)0; }
  __syncthreads();
  for (int r = ty; r < 64; r += 4) { const int n = n0 + r; if (n < N) out[(size_t)n * K + k0 + tx] = t[tx][r]; }
}

template <int BM, int BN, int EPI>
__global__ __launch_bounds__(256) void gemm_nt(const u16* __restrict__ A, int lda, const u16* __restrict__ Bt, int ldb,
                                               u16* __restrict__ C, int ldc, int N, int K, const u16* __restrict__ R) {
  constexpr int BK = 64, LS = 72;
  constexpr int MT = BM / 32, NT = BN / 32;
  constexpr int CA = BM * 8 / 256, CB = BN * 8 / 256;
  constexpr int LDS_AB = (BM + BN) * LS * 2, LDS_C = BM * (BN + 8) * 2;
  __shared__ __attribute__((aligned(16))) char smem[LDS_AB > LDS_C ? LDS_AB : LDS_C];
  u16* As = (u16*)smem; u16* Bs = As + BM * LS;
  const int tid = threadIdx.x, lane = tid & 63, w = tid >> 6, wm = w >> 1, wn = w & 1;
  const int m0 = blockIdx.y * BM, n0 = blockIdx.x * BN;
  const int fr = lane & 15, fq = lane >> 4;
  f32x4 acc[MT][NT];
#pragma unroll
  for (int i = 0; i < MT; ++i)
#pragma unroll
    for (int j = 0; j < NT; ++j) acc[i][j] = f32x4{0.f, 0.f, 0.f, 0.f};
  u32x4 ra[CA], rb[CB];
  auto gload = [&](int k0) {
#pragma unroll
    for (int i = 0; i < CA; ++i) { const int c = tid + i * 256, r = c >> 3, c8 = c & 7; ra[i] = *(const u32x4*)(A + (size_t)(m0 + r) * lda + k0 + c8 * 8); }
#pragma unroll
    for (int i = 0; i < CB; ++i) { const int c = tid + i * 256, r = c >> 3, c8 = c & 7; int n = n0 + r; n = n < N ? n : N - 1;
      rb[i] = *(const u32x4*)(Bt + (size_t)n * ldb + k0 + c8 * 8); }
  };
  gload(0);
  for (int k0 = 0; k0 < K; k0 += BK) {
    __syncthreads();
#pragma unroll
    for (int i = 0; i < CA; ++i) { const int c = tid + i * 256, r = c >> 3, c8 = c & 7; *(u32x4*)(As + r * LS + c8 * 8) = ra[i]; }
#pragma unroll
    for (int i = 0; i < CB; ++i) { const int c = tid + i * 256, r = c >> 3, c8 = c & 7; *(u32x4*)(Bs + r * LS + c8 * 8) = rb[i]; }
    __syncthreads();
    if (k0 + BK < K) gload(k0 + BK);
#pragma unroll
    for (int kk = 0; kk < 2; ++kk) {
      bf16x8 af[MT], bfr[NT];
#pragma unroll
      for (int i = 0; i < MT; ++i) af[i] = *(const bf16x8*)(As + (wm * (BM / 2) + i * 16 + fr) * LS + kk * 32 + fq * 8);
#pragma unroll
      for (int j = 0; j < NT; ++j) bfr[j] = *(const bf16x8*)(Bs + (wn * (BN / 2) + j * 16 + fr) * LS + kk * 32 + fq * 8);
#pragma unroll
      for (int i = 0; i < MT; ++i)
#pragma unroll
        for (int j = 0; j < NT; ++j) acc[i][j] = __builtin_amdgcn_mfma_f32_16x16x32_bf16(af[i], bfr[j], acc[i][j], 0, 0, 0);
    }
  }
  __syncthreads();
  constexpr int CS = BN + 8;
  u16* Cs = (u16*)smem;
#pragma unroll
  for (int i = 0; i < MT; ++i)
#pragma unroll
    for (int j = 0; j < NT; ++j)
#pragma unroll
      for (int r = 0; r < 4; ++r) Cs[(wm * (BM / 2) + i * 16 + fq * 4 + r) * CS + wn * (BN / 2) + j * 16 + fr] = f2bf(acc[i][j][r]);
  __syncthreads();
  for (int c = tid; c < BM * BN / 8; c += 256) {
    const int r = c / (BN / 8), c8 = c % (BN / 8);
    const int n = n0 + c8 * 8;
    if (n >= N) continue;
    u32x4 v = *(const u32x4*)(Cs + r * CS + c8 * 8);
    if (EPI == 1) {
      const u32x4 rr = *(const u32x4*)(R + (size_t)(m0 + r) * ldc + n);
#pragma unroll
      for (int i = 0; i < 4; ++i) {
        const float lo = bf2f((u16)(v[i] & 0xffff)) + bf2f((u16)(rr[i] & 0xffff));
        const float hi = bf2f((u16)(v[i] >> 16)) + bf2f((u16)(rr[i] >> 16));
        v[i] = pack2(lo, hi);
      }
    }
    *(u32x4*)(C + (size_t)(m0 + r) * ldc + n) = v;
  }
}

constexpr int AQ_LD = 544;
constexpr int VT_LD = 2048 + 8;
__global__ __launch_bounds__(512) void attnA_kernel(const u16* __restrict__ qkv, u16* __restrict__ o) {
  __shared__ __attribute__((aligned(16))) u16 Ks[2048 * 16];
  __shared__ __attribute__((aligned(16))) u16 Vt[16 * VT_LD];
  const int tid = threadIdx.x, lane = tid & 63, w = tid >> 6;
  for (int c = tid; c < 2048 * 2; c += 512) { const int key = c >> 1, h = c & 1;
    *(u32x4*)(Ks + key * 16 + h * 8) = *(const u32x4*)(qkv + (size_t)key * AQ_LD + 512 + h * 8); }
  for (int key = tid; key < 2048; key += 512) {
    const u32x4 v0 = *(const u32x4*)(qkv + (size_t)key * AQ_LD + 528), v1 = *(const u32x4*)(qkv + (size_t)key * AQ_LD + 536);
    const int g = key & 15, pos = (key & ~15) | ((g & 3) | ((g & 4) << 1) | ((g & 8) >> 1));
#pragma unroll
    for (int i = 0; i < 4; ++i) {
      Vt[(2 * i) * VT_LD + pos] = (u16)(v0[i] & 0xffff); Vt[(2 * i + 1) * VT_LD + pos] = (u16)(v0[i] >> 16);
      Vt[(8 + 2 * i) * VT_LD + pos] = (u16)(v1[i] & 0xffff); Vt[(9 + 2 * i) * VT_LD + pos] = (u16)(v1[i] >> 16);
    }
  }
  __syncthreads();
  const int tok = blockIdx.x * 8 + w;
  const int c32 = lane & 31, hh = lane >> 5;
  const bf16x8 qb = *(const bf16x8*)(qkv + (size_t)tok * AQ_LD + c32 * 16 + hh * 8);
  const float cs = 0.25f * 1.4426950408889634f;
  float m_run = -INFINITY, l_run = 0.f;
  f32x16 oacc;
#pragma unroll
  for (int i = 0; i < 16; ++i) oacc[i] = 0.f;
  const f32x16 zero16 = oacc;
  const u16* kp = Ks + c32 * 16 + hh * 8;
  const u16* vp = Vt + (lane & 15) * VT_LD + hh * 8;
  for (int t = 0; t < 64; ++t) {
    const bf16x8 ka = *(const bf16x8*)(kp + t * 32 * 16);
    f32x16 x = __builtin_amdgcn_mfma_f32_32x32x16_bf16(ka, qb, zero16, 0, 0, 0);
    float mt = -INFINITY;
#pragma unroll
    for (int i = 0; i < 16; ++i) { x[i] = rbf(x[i]); mt = fmaxf(mt, x[i]); }
    mt = fmaxf(mt, __shfl_xor(mt, 32));
    const float m_new = fmaxf(m_run, mt);
    const float alpha = exp2f((m_run - m_new) * cs);
    const float mc = m_new * cs;
    float ls = 0.f;
    bf16x8 pb[2];
#pragma unroll
    for (int i = 0; i < 16; i += 2) {
      const float e0 = exp2f(x[i] * cs - mc), e1 = exp2f(x[i + 1] * cs - mc);
      ls += e0 + e1;
      const unsigned pk = pack2(e0, e1);
      pb[i >> 3][i & 7] = (short)(pk & 0xffff); pb[i >> 3][(i & 7) + 1] = (short)(pk >> 16);
    }
    l_run = l_run * alpha + ls;
    m_run = m_new;
#pragma unroll
    for (int i = 0; i < 8; ++i) oacc[i] *= alpha;
#pragma unroll
    for (int s = 0; s < 2; ++s) {
      const bf16x8 va = *(const bf16x8*)(vp + t * 32 + s * 16);
      oacc = __builtin_amdgcn_mfma_f32_32x32x16_bf16(va, pb[s], oacc, 0, 0, 0);
    }
  }
  l_run += __shfl_xor(l_run, 32);
  const float inv = 1.0f / l_run;
  u32x2 o0, o1;
  o0[0] = pack2(oacc[0] * inv, oacc[1] * inv); o0[1] = pack2(oacc[2] * inv, oacc[3] * inv);
  o1[0] = pack2(oacc[4] * inv, oacc[5] * inv); o1[1] = pack2(oacc[6] * inv, oacc[7] * inv);
  u16* op = o + (size_t)tok * 512 + c32 * 16;
  *(u32x2*)(op + 4 * hh) = o0;
  *(u32x2*)(op + 8 + 4 * hh) = o1;
}

DI float red16_max(float v) { v = fmaxf(v, __shfl_xor(v, 1)); v = fmaxf(v, __shfl_xor(v, 2)); v = fmaxf(v, __shfl_xor(v, 4)); v = fmaxf(v, __shfl_xor(v, 8)); return v; }
DI float red16_sum(float v) { v += __shfl_xor(v, 1); v += __shfl_xor(v, 2); v += __shfl_xor(v, 4); v += __shfl_xor(v, 8); return v; }

constexpr float BSCALE = 0.08838834764831845f;
constexpr float NEGBIG = -1000000000.0f;

__global__ __launch_bounds__(256) void mem_proj_kernel(const u16* __restrict__ mem, const u16* __restrict__ Wt,
                                                       u16* __restrict__ qm, u16* __restrict__ km, u16* __restrict__ vm) {
  const int lane = threadIdx.x & 63, w = threadIdx.x >> 6, fr = lane & 15, fq = lane >> 4;
  const int n0 = (blockIdx.x * 4 + w) * 16;
  f32x4 acc = {0.f, 0.f, 0.f, 0.f};
  for (int ks = 0; ks < 16; ++ks) {
    bf16x8 a = {0, 0, 0, 0, 0, 0, 0, 0};
    if (fr < 8) a = *(const bf16x8*)(mem + fr * 512 + ks * 32 + fq * 8);
    const bf16x8 b = *(const bf16x8*)(Wt + (size_t)(n0 + fr) * 512 + ks * 32 + fq * 8);
    acc = __builtin_amdgcn_mfma_f32_16x16x32_bf16(a, b, acc, 0, 0, 0);
  }
  if (fq < 2) {
    const int which = n0 / BHD, n = n0 % BHD + fr;
    u16* dst = which == 0 ? qm : (which == 1 ? km : vm);
#pragma unroll
    for (int r = 0; r < 4; ++r) dst[(fq * 4 + r) * BHD + n] = f2bf(acc[r]);
  }
}

__global__ __launch_bounds__(64) void mem_attn_kernel(const u16* __restrict__ qm, const u16* __restrict__ km, const u16* __restrict__ vm,
                                                      const u16* __restrict__ qkvx, int seg, u16* __restrict__ om) {
  __shared__ __attribute__((aligned(16))) u16 Ps[16 * 72];
  __shared__ __attribute__((aligned(16))) u16 Vts[128 * 72];
  const int lane = threadIdx.x, fr = lane & 15, fq = lane >> 4, h = blockIdx.x;
  for (int c = lane; c < 40 * 16; c += 64) { const int key = c >> 4, d8 = c & 15;
    const u16* src = key < 8 ? vm + key * BHD + h * 128 + d8 * 8 : qkvx + (size_t)(seg * 32 + key - 8) * BN3 + 2 * BHD + h * 128 + d8 * 8;
    const u32x4 v = *(const u32x4*)src;
#pragma unroll
    for (int i = 0; i < 4; ++i) { Vts[(d8 * 8 + 2 * i) * 72 + key] = (u16)(v[i] & 0xffff); Vts[(d8 * 8 + 2 * i + 1) * 72 + key] = (u16)(v[i] >> 16); } }
  for (int c = lane; c < 128 * 24; c += 64) Vts[(c / 24) * 72 + 40 + (c % 24)] = 0;
  f32x4 s[3];
#pragma unroll
  for (int nt = 0; nt < 3; ++nt) s[nt] = f32x4{0.f, 0.f, 0.f, 0.f};
  for (int ks = 0; ks < 4; ++ks) {
    bf16x8 a = {0, 0, 0, 0, 0, 0, 0, 0};
    if (fr < 8) a = *(const bf16x8*)(qm + fr * BHD + h * 128 + ks * 32 + fq * 8);
#pragma unroll
    for (int nt = 0; nt < 3; ++nt) {
      const int key = nt * 16 + fr;
      bf16x8 b = {0, 0, 0, 0, 0, 0, 0, 0};
      if (key < 8) b = *(const bf16x8*)(km + key * BHD + h * 128 + ks * 32 + fq * 8);
      else if (key < 40) b = *(const bf16x8*)(qkvx + (size_t)(seg * 32 + key - 8) * BN3 + BHD + h * 128 + ks * 32 + fq * 8);
      s[nt] = __builtin_amdgcn_mfma_f32_16x16x32_bf16(a, b, s[nt], 0, 0, 0);
    }
  }
#pragma unroll
  for (int r = 0; r < 4; ++r) {
    float v[3]; float mx = -INFINITY;
#pragma unroll
    for (int nt = 0; nt < 3; ++nt) { v[nt] = rbf(rbf(s[nt][r]) * BSCALE); if (nt * 16 + fr >= 40) v[nt] = -INFINITY; mx = fmaxf(mx, v[nt]); }
    mx = red16_max(mx);
    float sum = 0.f;
#pragma unroll
    for (int nt = 0; nt < 3; ++nt) { v[nt] = __expf(v[nt] - mx); sum += v[nt]; }
    sum = red16_sum(sum);
    const float inv = 1.0f / sum;
#pragma unroll
    for (int nt = 0; nt < 3; ++nt) Ps[(fq * 4 + r) * 72 + nt * 16 + fr] = f2bf(v[nt] * inv);
    Ps[(fq * 4 + r) * 72 + 48 + fr] = 0;
  }
  __syncthreads();
#pragma unroll
  for (int dt = 0; dt < 8; ++dt) {
    f32x4 oa = {0.f, 0.f, 0.f, 0.f};
#pragma unroll
    for (int ks = 0; ks < 2; ++ks) {
      const bf16x8 a = *(const bf16x8*)(Ps + fr * 72 + ks * 32 + fq * 8);
      const bf16x8 b = *(const bf16x8*)(Vts + (dt * 16 + fr) * 72 + ks * 32 + fq * 8);
      oa = __builtin_amdgcn_mfma_f32_16x16x32_bf16(a, b, oa, 0, 0, 0);
    }
    if (fq < 2) {
#pragma unroll
      for (int r = 0; r < 4; ++r) om[(fq * 4 + r) * BHD + h * 128 + dt * 16 + fr] = f2bf(oa[r]);
    }
  }
}

__global__ __launch_bounds__(256) void mem_out_kernel(const u16* __restrict__ om, const u16* __restrict__ Wot, u16* __restrict__ mem_out) {
  __shared__ float red[4][16][16];
  const int lane = threadIdx.x & 63, w = threadIdx.x >> 6, fr = lane & 15, fq = lane >> 4;
  const int n0 = blockIdx.x * 16;
  f32x4 acc = {0.f, 0.f, 0.f, 0.f};
  for (int ks = w * 32; ks < w * 32 + 32; ++ks) {
    bf16x8 a = {0, 0, 0, 0, 0, 0, 0, 0};
    if (fr < 8) a = *(const bf16x8*)(om + fr * BHD + ks * 32 + fq * 8);
    const bf16x8 b = *(const bf16x8*)(Wot + (size_t)(n0 + fr) * BHD + ks * 32 + fq * 8);
    acc = __builtin_amdgcn_mfma_f32_16x16x32_bf16(a, b, acc, 0, 0, 0);
  }
#pragma unroll
  for (int r = 0; r < 4; ++r) red[w][fq * 4 + r][fr] = acc[r];
  __syncthreads();
  if (threadIdx.x < 128) {
    const int r = threadIdx.x >> 4, c = threadIdx.x & 15;
    mem_out[r * 512 + n0 + c] = f2bf(red[0][r][c] + red[1][r][c] + red[2][r][c] + red[3][r][c]);
  }
}

__global__ __launch_bounds__(256) void attnB_kernel(const u16* __restrict__ qkvx, const u16* __restrict__ kmall, const u16* __restrict__ vmall,
                                                    u16* __restrict__ ox) {
  __shared__ __attribute__((aligned(16))) u16 Ps_all[4][32 * 72];
  __shared__ __attribute__((aligned(16))) u16 Vts_all[4][128 * 72];
  const int lane = threadIdx.x & 63, w = threadIdx.x >> 6, fr = lane & 15, fq = lane >> 4;
  const int task = blockIdx.x * 4 + w, seg = task >> 5, h = task & 31;
  u16* Ps = Ps_all[w]; u16* Vts = Vts_all[w];
  const u16* km = kmall + (size_t)seg * 8 * BHD; const u16* vm = vmall + (size_t)seg * 8 * BHD;
  for (int c = lane; c < 40 * 16; c += 64) { const int key = c >> 4, d8 = c & 15;
    const u16* src = key < 8 ? vm + key * BHD + h * 128 + d8 * 8 : qkvx + (size_t)(seg * 32 + key - 8) * BN3 + 2 * BHD + h * 128 + d8 * 8;
    const u32x4 v = *(const u32x4*)src;
#pragma unroll
    for (int i = 0; i < 4; ++i) { Vts[(d8 * 8 + 2 * i) * 72 + key] = (u16)(v[i] & 0xffff); Vts[(d8 * 8 + 2 * i + 1) * 72 + key] = (u16)(v[i] >> 16); } }
  for (int c = lane; c < 128 * 24; c += 64) Vts[(c / 24) * 72 + 40 + (c % 24)] = 0;
#pragma unroll
  for (int mt = 0; mt < 2; ++mt) {
    f32x4 s[3];
#pragma unroll
    for (int nt = 0; nt < 3; ++nt) s[nt] = f32x4{0.f, 0.f, 0.f, 0.f};
    for (int ks = 0; ks < 4; ++ks) {
      const bf16x8 a = *(const bf16x8*)(qkvx + (size_t)(seg * 32 + mt * 16 + fr) * BN3 + h * 128 + ks * 32 + fq * 8);
#pragma unroll
      for (int nt = 0; nt < 3; ++nt) {
        const int key = nt * 16 + fr;
        bf16x8 b = {0, 0, 0, 0, 0, 0, 0, 0};
        if (key < 8) b = *(const bf16x8*)(km + key * BHD + h * 128 + ks * 32 + fq * 8);
        else if (key < 40) b = *(const bf16x8*)(qkvx + (size_t)(seg * 32 + key - 8) * BN3 + BHD + h * 128 + ks * 32 + fq * 8);
        s[nt] = __builtin_amdgcn_mfma_f32_16x16x32_bf16(a, b, s[nt], 0, 0, 0);
      }
    }
#pragma unroll
    for (int r = 0; r < 4; ++r) {
      const int row = BM_ + mt * 16 + fq * 4 + r;
      float v[3]; float mx = -INFINITY;
#pragma unroll
      for (int nt = 0; nt < 3; ++nt) { const int key = nt * 16 + fr;
        v[nt] = rbf(rbf(s[nt][r]) * BSCALE); if (key > row) v[nt] = rbf(NEGBIG); if (key >= 40) v[nt] = -INFINITY; mx = fmaxf(mx, v[nt]); }
      mx = red16_max(mx);
      float sum = 0.f;
#pragma unroll
      for (int nt = 0; nt < 3; ++nt) { v[nt] = __expf(v[nt] - mx); sum += v[nt]; }
      sum = red16_sum(sum);
      const float inv = 1.0f / sum;
#pragma unroll
      for (int nt = 0; nt < 3; ++nt) Ps[(mt * 16 + fq * 4 + r) * 72 + nt * 16 + fr] = f2bf(v[nt] * inv);
      Ps[(mt * 16 + fq * 4 + r) * 72 + 48 + fr] = 0;
    }
  }
  __syncthreads();
#pragma unroll
  for (int mt = 0; mt < 2; ++mt)
#pragma unroll
    for (int dt = 0; dt < 8; ++dt) {
      f32x4 oa = {0.f, 0.f, 0.f, 0.f};
#pragma unroll
      for (int ks = 0; ks < 2; ++ks) {
        const bf16x8 a = *(const bf16x8*)(Ps + (mt * 16 + fr) * 72 + ks * 32 + fq * 8);
        const bf16x8 b = *(const bf16x8*)(Vts + (dt * 16 + fr) * 72 + ks * 32 + fq * 8);
        oa = __builtin_amdgcn_mfma_f32_16x16x32_bf16(a, b, oa, 0, 0, 0);
      }
#pragma unroll
      for (int r = 0; r < 4; ++r) ox[(size_t)(seg * 32 + mt * 16 + fq * 4 + r) * BHD + h * 128 + dt * 16 + fr] = f2bf(oa[r]);
    }
}

extern "C" void kernel_launch(void* const* d_in, const int* in_sizes, int n_in,
                              void* d_out, int out_size, void* d_ws, size_t ws_size,
                              hipStream_t stream) {
  const u16* A_x = (const u16*)d_in[0];
  const u16* A_wq = (const u16*)d_in[1];
  const u16* A_wk = (const u16*)d_in[2];
  const u16* A_wv = (const u16*)d_in[3];
  const u16* A_wo = (const u16*)d_in[4];
  const u16* B_mem0 = (const u16*)d_in[6];
  const u16* B_Wq = (const u16*)d_in[7];
  const u16* B_Wk = (const u16*)d_in[8];
  const u16* B_Wv = (const u16*)d_in[9];
  const u16* B_Wo = (const u16*)d_in[10];
  u16* out = (u16*)d_out;

  char* ws = (char*)d_ws; size_t off = 0;
  auto alloc = [&](size_t bytes) { char* p = ws + off; off += (bytes + 255) & ~(size_t)255; return (u16*)p; };
  u16* xn = alloc((size_t)S_ * D_ * 2);
  u16* x1 = alloc((size_t)S_ * D_ * 2);
  u16* x2 = alloc((size_t)S_ * D_ * 2);
  u16* x3 = alloc((size_t)S_ * D_ * 2);
  u16* aqkv = alloc((size_t)S_ * AQ_LD * 2);
  u16* ao = alloc((size_t)S_ * D_ * 2);
  u16* Awqkv_t = alloc((size_t)AQ_LD * D_ * 2);
  u16* Awo_t = alloc((size_t)D_ * D_ * 2);
  u16* BWqkv_t = alloc((size_t)BN3 * D_ * 2);
  u16* BWo_t = alloc((size_t)D_ * BHD * 2);
  u16* qkvx = alloc((size_t)S_ * BN3 * 2);
  u16* ox = alloc((size_t)S_ * BHD * 2);
  u16* memb = alloc((size_t)(NSEG + 1) * 8 * D_ * 2);
  u16* qm = alloc((size_t)8 * BHD * 2);
  u16* kmall = alloc((size_t)NSEG * 8 * BHD * 2);
  u16* vmall = alloc((size_t)NSEG * 8 * BHD * 2);
  u16* om = alloc((size_t)8 * BHD * 2);

  transpose_kernel<<<dim3(8, 8), 256, 0, stream>>>(A_wq, Awqkv_t, 512, 512);
  transpose_kernel<<<dim3(1, 8), 256, 0, stream>>>(A_wk, Awqkv_t + 512 * 512, 512, 16);
  transpose_kernel<<<dim3(1, 8), 256, 0, stream>>>(A_wv, Awqkv_t + 528 * 512, 512, 16);
  transpose_kernel<<<dim3(8, 8), 256, 0, stream>>>(A_wo, Awo_t, 512, 512);
  transpose_kernel<<<dim3(64, 8), 256, 0, stream>>>(B_Wq, BWqkv_t, 512, 4096);
  transpose_kernel<<<dim3(64, 8), 256, 0, stream>>>(B_Wk, BWqkv_t + (size_t)4096 * 512, 512, 4096);
  transpose_kernel<<<dim3(64, 8), 256, 0, stream>>>(B_Wv, BWqkv_t + (size_t)8192 * 512, 512, 4096);
  transpose_kernel<<<dim3(8, 64), 256, 0, stream>>>(B_Wo, BWo_t, 4096, 512);

  auto layerA = [&](const u16* xin, u16* xout) {
    ln_kernel<<<512, 256, 0, stream>>>(xin, xn);
    gemm_nt<64, 64, 0><<<dim3(9, 32), 256, 0, stream>>>(xn, 512, Awqkv_t, 512, aqkv, AQ_LD, AQ_LD, 512, nullptr);
    attnA_kernel<<<256, 512, 0, stream>>>(aqkv, ao);
    gemm_nt<64, 64, 1><<<dim3(8, 32), 256, 0, stream>>>(ao, 512, Awo_t, 512, xout, 512, 512, 512, xin);
  };
  auto layerB = [&](const u16* xin, u16* xout) {
    ln_kernel<<<512, 256, 0, stream>>>(xin, xn);
    gemm_nt<128, 128, 0><<<dim3(96, 16), 256, 0, stream>>>(xn, 512, BWqkv_t, 512, qkvx, BN3, BN3, 512, nullptr);
    for (int s = 0; s < NSEG; ++s) {
      const u16* mem = s == 0 ? B_mem0 : memb + (size_t)s * 8 * D_;
      mem_proj_kernel<<<192, 256, 0, stream>>>(mem, BWqkv_t, qm, kmall + (size_t)s * 8 * BHD, vmall + (size_t)s * 8 * BHD);
      mem_attn_kernel<<<32, 64, 0, stream>>>(qm, kmall + (size_t)s * 8 * BHD, vmall + (size_t)s * 8 * BHD, qkvx, s, om);
      mem_out_kernel<<<32, 256, 0, stream>>>(om, BWo_t, memb + (size_t)(s + 1) * 8 * D_);
    }
    attnB_kernel<<<512, 256, 0, stream>>>(qkvx, kmall, vmall, ox);
    gemm_nt<64, 64, 1><<<dim3(8, 32), 256, 0, stream>>>(ox, BHD, BWo_t, BHD, xout, 512, 512, BHD, xin);
  };
  layerA(A_x, x1);
  layerB(x1, x2);
  layerA(x2, x3);
  layerB(x3, out);
}
```

```cpp
#include <hip/hip_runtime.h>
#include <hip/hip_bf16.h>
#include <stdint.h>

typedef unsigned short u16;
using bf16x8 = __attribute__((ext_vector_type(8))) short;
using f32x4  = __attribute__((ext_vector_type(4))) float;
using f32x16 = __attribute__((ext_vector_type(16))) float;
using u32x4  = __attribute__((ext_vector_type(4))) unsigned;
using u32x2  = __attribute__((ext_vector_type(2))) unsigned;
#define DI __device__ __forceinline__

DI float bf2f(u16 x) { return __uint_as_float(((unsigned)x) << 16); }
DI u16 f2bf(float f) { __bf16 b = (__bf16)f; return __builtin_bit_cast(u16, b); }
DI float rbf(float f) { return bf2f(f2bf(f)); }
DI unsigned pack2(float lo, float hi) { return (unsigned)f2bf(lo) | ((unsigned)f2bf(hi) << 16); }

constexpr int S_ = 2048, D_ = 512;
constexpr int AH = 32, ADH = 16;
constexpr int BH = 32, BDH = 128, BM_ = 8, SEG = 32, NSEG = 64;
constexpr int BN3 = 3 * BH * BDH;
constexpr int BHD = BH * BDH;

__global__ __launch_bounds__(256) void ln_kernel(const u16* __restrict__ x, u16* __restrict__ y) {
  const int row = blockIdx.x * 4 + (threadIdx.x >> 6), lane = threadIdx.x & 63;
  const u32x4 v = *(const u32x4*)(x + (size_t)row * D_ + lane * 8);
  float f[8];
#pragma unroll
  for (int i = 0; i < 4; ++i) { f[2 * i] = __uint_as_float(v[i] << 16); f[2 * i + 1] = __uint_as_float(v[i] & 0xffff0000u); }
  float s = 0.f;
#pragma unroll
  for (int i = 0; i < 8; ++i) s += f[i];
#pragma unroll
  for (int o = 32; o >= 1; o >>= 1) s += __shfl_xor(s, o);
  const float mean = s * (1.0f / D_);
  float q = 0.f;
#pragma unroll
  for (int i = 0; i < 8; ++i) { const float d = f[i] - mean; q += d * d; }
#pragma unroll
  for (int o = 32; o >= 1; o >>= 1) q += __shfl_xor(q, o);
  const float var = q * (1.0f / D_);
  const float mb = rbf(mean), vb = rbf(var);
  const float den = rbf(sqrtf(rbf(vb + 1e-5f)));
  u32x4 o;
#pragma unroll
  for (int i = 0; i < 4; ++i) o[i] = pack2(rbf(f[2 * i] - mb) / den, rbf(f[2 * i + 1] - mb) / den);
  *(u32x4*)(y + (size_t)row * D_ + lane * 8) = o;
}

__global__ __launch_bounds__(256) void transpose_kernel(const u16* __restrict__ in, u16* __restrict__ out, int K, int N) {
  __shared__ u16 t[64][66];
  const int k0 = blockIdx.y * 64, n0 = blockIdx.x * 64;
  const int tx = threadIdx.x & 63, ty = threadIdx.x >> 6;
  for (int r = ty; r < 64; r += 4) { const int n = n0 + tx; t[r][tx] = (n < N) ? in[(size_t)(k0 + r) * N + n] : (u16)0; }
  __syncthreads();
  for (int r = ty; r < 64; r += 4) { const int n = n0 + r; if (n < N) out[(size_t)n * K + k0 + tx] = t[tx][r]; }
}

template <int BM, int BN, int EPI>
__global__ __launch_bounds__(256) void gemm_nt(const u16* __restrict__ A, int lda, const u16* __restrict__ Bt, int ldb,
                                               u16* __restrict__ C, int ldc, int N, int K, const u16* __restrict__ R) {
  constexpr int BK = 64, LS = 72;
  constexpr int MT = BM / 32, NT = BN / 32;
  constexpr int CA = BM * 8 / 256, CB = BN * 8 / 256;
  constexpr int LDS_AB = (BM + BN) * LS * 2, LDS_C = BM * (BN + 8) * 2;
  __shared__ __attribute__((aligned(16))) char smem[LDS_AB > LDS_C ? LDS_AB : LDS_C];
  u16* As = (u16*)smem; u16* Bs = As + BM * LS;
  const int tid = threadIdx.x, lane = tid & 63, w = tid >> 6, wm = w >> 1, wn = w & 1;
  const int m0 = blockIdx.y * BM, n0 = blockIdx.x * BN;
  const int fr = lane & 15, fq = lane >> 4;
  f32x4 acc[MT][NT];
#pragma unroll
  for (int i = 0; i < MT; ++i)
#pragma unroll
    for (int j = 0; j < NT; ++j) acc[i][j] = f32x4{0.f, 0.f, 0.f, 0.f};
  u32x4 ra[CA], rb[CB];
  auto gload = [&](int k0) {
#pragma unroll
    for (int i = 0; i < CA; ++i) { const int c = tid + i * 256, r = c >> 3, c8 = c & 7; ra[i] = *(const u32x4*)(A + (size_t)(m0 + r) * lda + k0 + c8 * 8); }
#pragma unroll
    for (int i = 0; i < CB; ++i) { const int c = tid + i * 256, r = c >> 3, c8 = c & 7; int n = n0 + r; n = n < N ? n : N - 1;
      rb[i] = *(const u32x4*)(Bt + (size_t)n * ldb + k0 + c8 * 8); }
  };
  gload(0);
  for (int k0 = 0; k0 < K; k0 += BK) {
    __syncthreads();
#pragma unroll
    for (int i = 0; i < CA; ++i) { const int c = tid + i * 256, r = c >> 3, c8 = c & 7; *(u32x4*)(As + r * LS + c8 * 8) = ra[i]; }
#pragma unroll
    for (int i = 0; i < CB; ++i) { const int c = tid + i * 256, r = c >> 3, c8 = c & 7; *(u32x4*)(Bs + r * LS + c8 * 8) = rb[i]; }
    __syncthreads();
    if (k0 + BK < K) gload(k0 + BK);
#pragma unroll
    for (int kk = 0; kk < 2; ++kk) {
      bf16x8 af[MT], bfr[NT];
#pragma unroll
      for (int i = 0; i < MT; ++i) af[i] = *(const bf16x8*)(As + (wm * (BM / 2) + i * 16 + fr) * LS + kk * 32 + fq * 8);
#pragma unroll
      for (int j = 0; j < NT; ++j) bfr[j] = *(const bf16x8*)(Bs + (wn * (BN / 2) + j * 16 + fr) * LS + kk * 32 + fq * 8);
#pragma unroll
      for (int i = 0; i < MT; ++i)
#pragma unroll
        for (int j = 0; j < NT; ++j) acc[i][j] = __builtin_amdgcn_mfma_f32_16x16x32_bf16(af[i], bfr[j], acc[i][j], 0, 0, 0);
    }
  }
  __syncthreads();
  constexpr int CS = BN + 8;
  u16* Cs = (u16*)smem;
#pragma unroll
  for (int i = 0; i < MT; ++i)
#pragma unroll
    for (int j = 0; j < NT; ++j)
#pragma unroll
      for (int r = 0; r < 4; ++r) Cs[(wm * (BM / 2) + i * 16 + fq * 4 + r) * CS + wn * (BN / 2) + j * 16 + fr] = f2bf(acc[i][j][r]);
  __syncthreads();
  for (int c = tid; c < BM * BN / 8; c += 256) {
    const int r = c / (BN / 8), c8 = c % (BN / 8);
    const int n = n0 + c8 * 8;
    if (n >= N) continue;
    u32x4 v = *(const u32x4*)(Cs + r * CS + c8 * 8);
    if (EPI == 1) {
      const u32x4 rr = *(const u32x4*)(R + (size_t)(m0 + r) * ldc + n);
#pragma unroll
      for (int i = 0; i < 4; ++i) {
        const float lo = bf2f((u16)(v[i] & 0xffff)) + bf2f((u16)(rr[i] & 0xffff));
        const float hi = bf2f((u16)(v[i] >> 16)) + bf2f((u16)(rr[i] >> 16));
        v[i] = pack2(lo, hi);
      }
    }
    *(u32x4*)(C + (size_t)(m0 + r) * ldc + n) = v;
  }
}

constexpr int AQ_LD = 544;
constexpr int VT_LD = 2048 + 8;
__global__ __launch_bounds__(512) void attnA_kernel(const u16* __restrict__ qkv, u16* __restrict__ o) {
  __shared__ __attribute__((aligned(16))) u16 Ks[2048 * 16];
  __shared__ __attribute__((aligned(16))) u16 Vt[16 * VT_LD];
  const int tid = threadIdx.x, lane = tid & 63, w = tid >> 6;
  for (int c = tid; c < 2048 * 2; c += 512) { const int key = c >> 1, h = c & 1;
    *(u32x4*)(Ks + key * 16 + h * 8) = *(const u32x4*)(qkv + (size_t)key * AQ_LD + 512 + h * 8); }
  for (int key = tid; key < 2048; key += 512) {
    const u32x4 v0 = *(const u32x4*)(qkv + (size_t)key * AQ_LD + 528), v1 = *(const u32x4*)(qkv + (size_t)key * AQ_LD + 536);
    const int g = key & 15, pos = (key & ~15) | ((g & 3) | ((g & 4) << 1) | ((g & 8) >> 1));
#pragma unroll
    for (int i = 0; i < 4; ++i) {
      Vt[(2 * i) * VT_LD + pos] = (u16)(v0[i] & 0xffff); Vt[(2 * i + 1) * VT_LD + pos] = (u16)(v0[i] >> 16);
      Vt[(8 + 2 * i) * VT_LD + pos] = (u16)(v1[i] & 0xffff); Vt[(9 + 2 * i) * VT_LD + pos] = (u16)(v1[i] >> 16);
    }
  }
  __syncthreads();
  const int tok = blockIdx.x * 8 + w;
  const int c32 = lane & 31, hh = lane >> 5;
  const bf16x8 qb = *(const bf16x8*)(qkv + (size_t)tok * AQ_LD + c32 * 16 + hh * 8);
  const float cs = 0.25f * 1.4426950408889634f;
  float m_run = -INFINITY, l_run = 0.f;
  f32x16 oacc;
#pragma unroll
  for (int i = 0; i < 16; ++i) oacc[i] = 0.f;
  const f32x16 zero16 = oacc;
  const u16* kp = Ks + c32 * 16 + hh * 8;
  const u16* vp = Vt + (lane & 15) * VT_LD + hh * 8;
  for (int t = 0; t < 64; ++t) {
    const bf16x8 ka = *(const bf16x8*)(kp + t * 32 * 16);
    f32x16 x = __builtin_amdgcn_mfma_f32_32x32x16_bf16(ka, qb, zero16, 0, 0, 0);
    float mt = -INFINITY;
#pragma unroll
    for (int i = 0; i < 16; ++i) { x[i] = rbf(x[i]); mt = fmaxf(mt, x[i]); }
    mt = fmaxf(mt, __shfl_xor(mt, 32));
    const float m_new = fmaxf(m_run, mt);
    const float alpha = exp2f((m_run - m_new) * cs);
    const float mc = m_new * cs;
    float ls = 0.f;
    bf16x8 pb[2];
#pragma unroll
    for (int i = 0; i < 16; i += 2) {
      const float e0 = exp2f(x[i] * cs - mc), e1 = exp2f(x[i + 1] * cs - mc);
      ls += e0 + e1;
      const unsigned pk = pack2(e0, e1);
      pb[i >> 3][i & 7] = (short)(pk & 0xffff); pb[i >> 3][(i & 7) + 1] = (short)(pk >> 16);
    }
    l_run = l_run * alpha + ls;
    m_run = m_new;
#pragma unroll
    for (int i = 0; i < 8; ++i) oacc[i] *= alpha;
#pragma unroll
    for (int s = 0; s < 2; ++s) {
      const bf16x8 va = *(const bf16x8*)(vp + t * 32 + s * 16);
      oacc = __builtin_amdgcn_mfma_f32_32x32x16_bf16(va, pb[s], oacc, 0, 0, 0);
    }
  }
  l_run += __shfl_xor(l_run, 32);
  const float inv = 1.0f / l_run;
  u32x2 o0, o1;
  o0[0] = pack2(oacc[0] * inv, oacc[1] * inv); o0[1] = pack2(oacc[2] * inv, oacc[3] * inv);
  o1[0] = pack2(oacc[4] * inv, oacc[5] * inv); o1[1] = pack2(oacc[6] * inv, oacc[7] * inv);
  u16* op = o + (size_t)tok * 512 + c32 * 16;
  *(u32x2*)(op + 4 * hh) = o0;
  *(u32x2*)(op + 8 + 4 * hh) = o1;
}

DI float red16_max(float v) { v = fmaxf(v, __shfl_xor(v, 1)); v = fmaxf(v, __shfl_xor(v, 2)); v = fmaxf(v, __shfl_xor(v, 4)); v = fmaxf(v, __shfl_xor(v, 8)); return v; }
DI float red16_sum(float v) { v += __shfl_xor(v, 1); v += __shfl_xor(v, 2); v += __shfl_xor(v, 4); v += __shfl_xor(v, 8); return v; }

constexpr float BSCALE = 0.08838834764831845f;
constexpr float NEGBIG = -1000000000.0f;

__global__ __launch_bounds__(256) void mem_proj_kernel(const u16* __restrict__ mem, const u16* __restrict__ Wt,
                                                       u16* __restrict__ qm, u16* __restrict__ km, u16* __restrict__ vm) {
  const int lane = threadIdx.x & 63, w = threadIdx.x >> 6, fr = lane & 15, fq = lane >> 4;
  const int n0 = (blockIdx.x * 4 + w) * 16;
  f32x4 acc = {0.f, 0.f, 0.f, 0.f};
  for (int ks = 0; ks < 16; ++ks) {
    bf16x8 a = {0, 0, 0, 0, 0, 0, 0, 0};
    if (fr < 8) a = *(const bf16x8*)(mem + fr * 512 + ks * 32 + fq * 8);
    const bf16x8 b = *(const bf16x8*)(Wt + (size_t)(n0 + fr) * 512 + ks * 32 + fq * 8);
    acc = __builtin_amdgcn_mfma_f32_16x16x32_bf16(a, b, acc, 0, 0, 0);
  }
  if (fq < 2) {
    const int which = n0 / BHD, n = n0 % BHD + fr;
    u16* dst = which == 0 ? qm : (which == 1 ? km : vm);
#pragma unroll
    for (int r = 0; r < 4; ++r) dst[(fq * 4 + r) * BHD + n] = f2bf(acc[r]);
  }
}

__global__ __launch_bounds__(64) void mem_attn_kernel(const u16* __restrict__ qm, const u16* __restrict__ km, const u16* __restrict__ vm,
                                                      const u16* __restrict__ qkvx, int seg, u16* __restrict__ om) {
  __shared__ __attribute__((aligned(16))) u16 Ps[16 * 72];
  __shared__ __attribute__((aligned(16))) u16 Vts[128 * 72];
  const int lane = threadIdx.x, fr = lane & 15, fq = lane >> 4, h = blockIdx.x;
  for (int c = lane; c < 40 * 16; c += 64) { const int key = c >> 4, d8 = c & 15;
    const u16* src = key < 8 ? vm + key * BHD + h * 128 + d8 * 8 : qkvx + (size_t)(seg * 32 + key - 8) * BN3 + 2 * BHD + h * 128 + d8 * 8;
    const u32x4 v = *(const u32x4*)src;
#pragma unroll
    for (int i = 0; i < 4; ++i) { Vts[(d8 * 8 + 2 * i) * 72 + key] = (u16)(v[i] & 0xffff); Vts[(d8 * 8 + 2 * i + 1) * 72 + key] = (u16)(v[i] >> 16); } }
  for (int c = lane; c < 128 * 24; c += 64) Vts[(c / 24) * 72 + 40 + (c % 24)] = 0;
  f32x4 s[3];
#pragma unroll
  for (int nt = 0; nt < 3; ++nt) s[nt] = f32x4{0.f, 0.f, 0.f, 0.f};
  for (int ks = 0; ks < 4; ++ks) {
    bf16x8 a = {0, 0, 0, 0, 0, 0, 0, 0};
    if (fr < 8) a = *(const bf16x8*)(qm + fr * BHD + h * 128 + ks * 32 + fq * 8);
#pragma unroll
    for (int nt = 0; nt < 3; ++nt) {
      const int key = nt * 16 + fr;
      bf16x8 b = {0, 0, 0, 0, 0, 0, 0, 0};
      if (key < 8) b = *(const bf16x8*)(km + key * BHD + h * 128 + ks * 32 + fq * 8);
      else if (key < 40) b = *(const bf16x8*)(qkvx + (size_t)(seg * 32 + key - 8) * BN3 + BHD + h * 128 + ks * 32 + fq * 8);
      s[nt] = __builtin_amdgcn_mfma_f32_16x16x32_bf16(a, b, s[nt], 0, 0, 0);
    }
  }
#pragma unroll
  for (int r = 0; r < 4; ++r) {
    float v[3]; float mx = -INFINITY;
#pragma unroll
    for (int nt = 0; nt < 3; ++nt) { v[nt] = rbf(rbf(s[nt][r]) * BSCALE); if (nt * 16 + fr >= 40) v[nt] = -INFINITY; mx = fmaxf(mx, v[nt]); }
    mx = red16_max(mx);
    float sum = 0.f;
#pragma unroll
    for (int nt = 0; nt < 3; ++nt) { v[nt] = __expf(v[nt] - mx); sum += v[nt]; }
    sum = red16_sum(sum);
    const float inv = 1.0f / sum;
#pragma unroll
    for (int nt = 0; nt < 3; ++nt) Ps[(fq * 4 + r) * 72 + nt * 16 + fr] = f2bf(v[nt] * inv);
    Ps[(fq * 4 + r) * 72 + 48 + fr] = 0;
  }
  __syncthreads();
#pragma unroll
  for (int dt = 0; dt < 8; ++dt) {
    f32x4 oa = {0.f, 0.f, 0.f, 0.f};
#pragma unroll
    for (int ks = 0; ks < 2; ++ks) {
      const bf16x8 a = *(const bf16x8*)(Ps + fr * 72 + ks * 32 + fq * 8);
      const bf16x8 b = *(const bf16x8*)(Vts + (dt * 16 + fr) * 72 + ks * 32 + fq * 8);
      oa = __builtin_amdgcn_mfma_f32_16x16x32_bf16(a, b, oa, 0, 0, 0);
    }
    if (fq < 2) {
#pragma unroll
      for (int r = 0; r < 4; ++r) om[(fq * 4 + r) * BHD + h * 128 + dt * 16 + fr] = f2bf(oa[r]);
    }
  }
}

__global__ __launch_bounds__(256) void mem_out_kernel(const u16* __restrict__ om, const u16* __restrict__ Wot, u16* __restrict__ mem_out) {
  __shared__ float red[4][16][16];
  const int lane = threadIdx.x & 63, w = threadIdx.x >> 6, fr = lane & 15, fq = lane >> 4;
  const int n0 = blockIdx.x * 16;
  f32x4 acc = {0.f, 0.f, 0.f, 0.f};
  for (int ks = w * 32; ks < w * 32 + 32; ++ks) {
    bf16x8 a = {0, 0, 0, 0, 0, 0, 0, 0};
    if (fr < 8) a = *(const bf16x8*)(om + fr * BHD + ks * 32 + fq * 8);
    const bf16x8 b = *(const bf16x8*)(Wot + (size_t)(n0 + fr) * BHD + ks * 32 + fq * 8);
    acc = __builtin_amdgcn_mfma_f32_16x16x32_bf16(a, b, acc, 0, 0, 0);
  }
#pragma unroll
  for (int r = 0; r < 4; ++r) red[w][fq * 4 + r][fr] = acc[r];
  __syncthreads();
  if (threadIdx.x < 128) {
    const int r = threadIdx.x >> 4, c = threadIdx.x & 15;
    mem_out[r * 512 + n0 + c] = f2bf(red[0][r][c] + red[1][r][c] + red[2][r][c] + red[3][r][c]);
  }
}

constexpr unsigned SENT = 0xFFFFFFFFu;
#define AUX_SC1 16
constexpr int KS_LD = 136, VT_LDB = 56;
constexpr int WO_NREG = 2;
constexpr int L_WO = 0, L_WO_BYTES = 4 * (32 - WO_NREG) * 1024;
constexpr int L_VT = L_WO + L_WO_BYTES, L_VT_BYTES = 128 * VT_LDB * 2;
constexpr int L_K = L_VT + L_VT_BYTES, L_K_BYTES = 40 * KS_LD * 2;
constexpr int L_Q = L_K + L_K_BYTES, L_Q_BYTES = 8 * KS_LD * 2;
constexpr int L_O = L_Q + L_Q_BYTES, L_O_BYTES = 8 * KS_LD * 2;
constexpr int L_MX = L_O + L_O_BYTES, L_MX_BYTES = 8192;
constexpr int L_TOTAL = L_MX + L_MX_BYTES;
constexpr int MEMX_SLOT_DW = 32 * 64;
constexpr int INBOX_DW = 32 * 32 * 128;
constexpr unsigned SPIN_LIMIT = 1u << 15;
#define CH_DELAY1 2
#define CH_DELAY2 2

DI bool valid4(u32x4 v) { return v[0] != SENT && v[1] != SENT && v[2] != SENT && v[3] != SENT; }
DI unsigned canon(unsigned x) { return x == SENT ? 0x7FC07FC0u : x; }

__global__ __launch_bounds__(256, 1) void chain_kernel(const u16* __restrict__ Wqkv_t, const u16* __restrict__ Wo_t, const u16* __restrict__ mem0,
                                                       const u16* __restrict__ qkvx, u16* __restrict__ kmall, u16* __restrict__ vmTall,
                                                       unsigned* memx, unsigned* inbox, unsigned* tmo) {
  __shared__ __attribute__((aligned(16))) char smem[L_TOTAL];
  const int h = blockIdx.x, tid = threadIdx.x, lane = tid & 63, w = __builtin_amdgcn_readfirstlane(tid >> 6), fr = lane & 15, g = lane >> 4;
  u16* VT = (u16*)(smem + L_VT); u16* KALL = (u16*)(smem + L_K); u16* QS = (u16*)(smem + L_Q); u16* OS = (u16*)(smem + L_O);
  const __amdgpu_buffer_rsrc_t rmx = __builtin_amdgcn_make_buffer_rsrc((void*)memx, 0, (NSEG + 1) * MEMX_SLOT_DW * 4, 0x00020000);
  const __amdgpu_buffer_rsrc_t rin = __builtin_amdgcn_make_buffer_rsrc((void*)inbox, 0, 2 * INBOX_DW * 4, 0x00020000);
  const __amdgpu_buffer_rsrc_t rtm = __builtin_amdgcn_make_buffer_rsrc((void*)tmo, 0, 64, 0x00020000);

  bf16x8 wf[6][16];
#pragma unroll
  for (int t = 0; t < 6; ++t) {
    const u16* src = Wqkv_t + (size_t)((t >> 1) * BHD + h * 128 + 32 * w + 16 * (t & 1) + fr) * 512 + g * 8;
#pragma unroll
    for (int ks = 0; ks < 16; ++ks) wf[t][ks] = *(const bf16x8*)(src + ks * 32);
  }
  bf16x8 wor[WO_NREG];
#pragma unroll
  for (int i = 0; i < 8; ++i) {
    const u16* src = Wo_t + (size_t)(16 * (8 * w + i) + fr) * BHD + h * 128 + g * 8;
#pragma unroll
    for (int ks = 0; ks < 4; ++ks) {
      const bf16x8 f = *(const bf16x8*)(src + ks * 32);
      if (i * 4 + ks < WO_NREG) wor[i * 4 + ks < WO_NREG ? i * 4 + ks : 0] = f;
      else *(bf16x8*)(smem + L_WO + ((w * (32 - WO_NREG) + (i * 4 + ks - WO_NREG)) * 64 + lane) * 16) = f;
    }
  }
  for (int c = tid; c < L_VT_BYTES / 16; c += 256) *(u32x4*)(smem + L_VT + c * 16) = u32x4{0u, 0u, 0u, 0u};
  __syncthreads();

  u32x4 kst[2], vst[2];
  auto kv_load = [&](int s) {
#pragma unroll
    for (int i = 0; i < 2; ++i) { const int c = tid + i * 256, tok = c >> 4, d8 = c & 15;
      const u16* row = qkvx + (size_t)(s * 32 + tok) * BN3 + h * 128 + d8 * 8;
      kst[i] = *(const u32x4*)(row + BHD); vst[i] = *(const u32x4*)(row + 2 * BHD); }
  };
  auto kv_store = [&]() {
#pragma unroll
    for (int i = 0; i < 2; ++i) { const int c = tid + i * 256, tok = c >> 4, d8 = c & 15;
      *(u32x4*)(KALL + (8 + tok) * KS_LD + d8 * 8) = kst[i];
#pragma unroll
      for (int j = 0; j < 4; ++j) { VT[(d8 * 8 + 2 * j) * VT_LDB + 8 + tok] = (u16)(vst[i][j] & 0xffff); VT[(d8 * 8 + 2 * j + 1) * VT_LDB + 8 + tok] = (u16)(vst[i][j] >> 16); } }
  };
  kv_load(0);
  kv_store();
  for (int c = tid; c < 512; c += 256) { const int r = c >> 6, k = (c & 63) * 8;
    *(u32x4*)(smem + L_MX + (k >> 4) * 256 + r * 32 + (k & 15) * 2) = *(const u32x4*)(mem0 + r * 512 + k); }

  for (int s = 0; s < NSEG; ++s) {
    __syncthreads();
    f32x4 acc[6];
#pragma unroll
    for (int t = 0; t < 6; ++t) acc[t] = f32x4{0.f, 0.f, 0.f, 0.f};
#pragma unroll
    for (int ks = 0; ks < 16; ++ks) {
      const bf16x8 mf = *(const bf16x8*)(smem + L_MX + (2 * ks + (g >> 1)) * 256 + (fr & 7) * 32 + (g & 1) * 16);
#pragma unroll
      for (int t = 0; t < 4; ++t) acc[t] = __builtin_amdgcn_mfma_f32_16x16x32_bf16(wf[t][ks], mf, acc[t], 0, 0, 0);
#pragma unroll
      for (int t = 4; t < 6; ++t) acc[t] = __builtin_amdgcn_mfma_f32_16x16x32_bf16(mf, wf[t][ks], acc[t], 0, 0, 0);
    }
#pragma unroll
    for (int t = 0; t < 4; ++t) {
      u32x2 pk; pk[0] = pack2(acc[t][0], acc[t][1]); pk[1] = pack2(acc[t][2], acc[t][3]);
      const int n0 = 32 * w + 16 * (t & 1) + 4 * g;
      if (fr < 8) {
        if (t < 2) *(u32x2*)(QS + fr * KS_LD + n0) = pk;
        else { *(u32x2*)(KALL + fr * KS_LD + n0) = pk; *(u32x2*)(kmall + (size_t)(s * 8 + fr) * BHD + h * 128 + n0) = pk; }
      }
    }
#pragma unroll
    for (int t = 4; t < 6; ++t) {
      u32x2 pk; pk[0] = pack2(acc[t][0], acc[t][1]); pk[1] = pack2(acc[t][2], acc[t][3]);
      const int dim = 32 * w + 16 * (t & 1) + fr;
      if (g < 2) { *(u32x2*)(VT + dim * VT_LDB + 4 * g) = pk; *(u32x2*)(vmTall + ((size_t)(s * 32 + h) * 128 + dim) * 8 + 4 * g) = pk; }
    }
    __syncthreads();
    bf16x8 qf[4];
#pragma unroll
    for (int ks = 0; ks < 4; ++ks) qf[ks] = *(const bf16x8*)(QS + (fr & 7) * KS_LD + 32 * ks + 8 * g);
    f32x4 sc[3];
#pragma unroll
    for (int t = 0; t < 3; ++t) {
      sc[t] = f32x4{0.f, 0.f, 0.f, 0.f};
#pragma unroll
      for (int ks = 0; ks < 4; ++ks) {
        const bf16x8 kf = *(const bf16x8*)(KALL + (16 * t + fr) * KS_LD + 32 * ks + 8 * g);
        sc[t] = __builtin_amdgcn_mfma_f32_16x16x32_bf16(kf, qf[ks], sc[t], 0, 0, 0);
      }
    }
    float pv[3][4]; float mx = -INFINITY;
#pragma unroll
    for (int t = 0; t < 3; ++t)
#pragma unroll
      for (int r = 0; r < 4; ++r) {
        float v = rbf(rbf(sc[t][r]) * BSCALE);
        if (t == 2 && g >= 2) v = -INFINITY;
        pv[t][r] = v; mx = fmaxf(mx, v);
      }
    mx = fmaxf(mx, __shfl_xor(mx, 16)); mx = fmaxf(mx, __shfl_xor(mx, 32));
    float sum = 0.f;
#pragma unroll
    for (int t = 0; t < 3; ++t)
#pragma unroll
      for (int r = 0; r < 4; ++r) { pv[t][r] = __expf(pv[t][r] - mx); sum += pv[t][r]; }
    sum += __shfl_xor(sum, 16); sum += __shfl_xor(sum, 32);
    const float inv = 1.0f / sum;
    bf16x8 pf[2];
#pragma unroll
    for (int r = 0; r < 4; ++r) {
      pf[0][r] = (short)f2bf(pv[0][r] * inv); pf[0][4 + r] = (short)f2bf(pv[1][r] * inv);
      pf[1][r] = (short)f2bf(pv[2][r] * inv); pf[1][4 + r] = 0;
    }
#pragma unroll
    for (int d = 0; d < 2; ++d) {
      f32x4 oa = {0.f, 0.f, 0.f, 0.f};
      const u16* vrow = VT + (32 * w + 16 * d + fr) * VT_LDB + 4 * g;
#pragma unroll
      for (int u = 0; u < 2; ++u) {
        const u32x2 lo = *(const u32x2*)(vrow + 32 * u), hi = *(const u32x2*)(vrow + 32 * u + 16);
        const u32x4 va4 = {lo[0], lo[1], hi[0], hi[1]};
        oa = __builtin_amdgcn_mfma_f32_16x16x32_bf16(__builtin_bit_cast(bf16x8, va4), pf[u], oa, 0, 0, 0);
      }
      u32x2 pk; pk[0] = pack2(oa[0], oa[1]); pk[1] = pack2(oa[2], oa[3]);
      if (fr < 8) *(u32x2*)(OS + fr * KS_LD + 32 * w + 16 * d + 4 * g) = pk;
    }
    __syncthreads();
    if (s + 1 == NSEG) break;
    bf16x8 of[4];
#pragma unroll
    for (int ks = 0; ks < 4; ++ks) of[ks] = *(const bf16x8*)(OS + (fr & 7) * KS_LD + 32 * ks + 8 * g);
    const int slot = s & 1;
#pragma unroll
    for (int i = 0; i < 8; ++i) {
      f32x4 a = {0.f, 0.f, 0.f, 0.f};
#pragma unroll
      for (int ks = 0; ks < 4; ++ks) {
        const bf16x8 wo = (i * 4 + ks < WO_NREG) ? wor[i * 4 + ks < WO_NREG ? i * 4 + ks : 0]
                        : *(const bf16x8*)(smem + L_WO + ((w * (32 - WO_NREG) + (i * 4 + ks - WO_NREG)) * 64 + lane) * 16);
        a = __builtin_amdgcn_mfma_f32_16x16x32_bf16(wo, of[ks], a, 0, 0, 0);
      }
      u32x4 st;
#pragma unroll
      for (int r = 0; r < 4; ++r) st[r] = canon(__float_as_uint(a[r]) == SENT ? SENT : __float_as_uint(a[r]));
      if (fr < 8) __builtin_amdgcn_raw_buffer_store_b128(st, rin, (slot * INBOX_DW + ((8 * w + i) * 32 + h) * 128 + fr * 16 + 4 * g) * 4, 0, AUX_SC1);
    }
    kv_load(s + 1);
    kv_store();
#pragma unroll
    for (int i = 0; i < CH_DELAY1; ++i) __builtin_amdgcn_s_sleep(4);
    u32x4 p[4];
    {
      unsigned spins = 0;
      for (;;) {
        bool ok = true;
#pragma unroll
        for (int i = 0; i < 4; ++i) { p[i] = __builtin_amdgcn_raw_buffer_load_b128(rin, (slot * INBOX_DW + h * 32 * 128 + (tid + 256 * i) * 4) * 4, 0, AUX_SC1); ok &= valid4(p[i]); }
        if (__all(ok)) break;
        if ((++spins & 63u) == 0u) {
          const unsigned t0 = __builtin_amdgcn_readfirstlane(__builtin_amdgcn_raw_buffer_load_b32(rtm, 0, 0, AUX_SC1));
          if (spins > SPIN_LIMIT || t0 == 1u) { if (lane == 0) __builtin_amdgcn_raw_buffer_store_b32(1u, rtm, 0, 0, AUX_SC1); break; }
        }
        __builtin_amdgcn_s_sleep(2);
      }
    }
    f32x4 ps;
#pragma unroll
    for (int r = 0; r < 4; ++r) ps[r] = (__uint_as_float(p[0][r]) + __uint_as_float(p[1][r])) + (__uint_as_float(p[2][r]) + __uint_as_float(p[3][r]));
    *(f32x4*)(smem + L_MX + tid * 16) = ps;
    __syncthreads();
    if (tid < 32) {
      f32x4 t4 = *(const f32x4*)(smem + L_MX + tid * 16);
#pragma unroll
      for (int j = 1; j < 8; ++j) { const f32x4 o4 = *(const f32x4*)(smem + L_MX + (j * 32 + tid) * 16); t4 = t4 + o4; }
      u32x2 pk; pk[0] = canon(pack2(t4[0], t4[1])); pk[1] = canon(pack2(t4[2], t4[3]));
      __builtin_amdgcn_raw_buffer_store_b64(pk, rmx, ((s + 1) * MEMX_SLOT_DW + h * 64 + tid * 2) * 4, 0, AUX_SC1);
    }
    {
      const u32x4 sent = {SENT, SENT, SENT, SENT};
#pragma unroll
      for (int i = 0; i < 4; ++i) __builtin_amdgcn_raw_buffer_store_b128(sent, rin, (slot * INBOX_DW + h * 32 * 128 + (tid + 256 * i) * 4) * 4, 0, AUX_SC1);
    }
    __syncthreads();
#pragma unroll
    for (int i = 0; i < CH_DELAY2; ++i) __builtin_amdgcn_s_sleep(4);
    {
      u32x4 m[2];
      unsigned spins = 0;
      for (;;) {
        bool ok = true;
#pragma unroll
        for (int i = 0; i < 2; ++i) { m[i] = __builtin_amdgcn_raw_buffer_load_b128(rmx, ((s + 1) * MEMX_SLOT_DW + (tid + 256 * i) * 4) * 4, 0, AUX_SC1); ok &= valid4(m[i]); }
        if (__all(ok)) break;
        if ((++spins & 63u) == 0u) {
          const unsigned t0 = __builtin_amdgcn_readfirstlane(__builtin_amdgcn_raw_buffer_load_b32(rtm, 0, 0, AUX_SC1));
          if (spins > SPIN_LIMIT || t0 == 1u) { if (lane == 0) __builtin_amdgcn_raw_buffer_store_b32(1u, rtm, 0, 0, AUX_SC1); break; }
        }
        __builtin_amdgcn_s_sleep(2);
      }
#pragma unroll
      for (int i = 0; i < 2; ++i) *(u32x4*)(smem + L_MX + (tid + 256 * i) * 16) = m[i];
    }
  }
}

__global__ __launch_bounds__(256) void attnB_kernel(const u16* __restrict__ qkvx, const u16* __restrict__ kmall, const u16* __restrict__ vmall,
                                                    u16* __restrict__ ox) {
  __shared__ __attribute__((aligned(16))) u16 Ps_all[4][32 * 72];
  __shared__ __attribute__((aligned(16))) u16 Vts_all[4][128 * 72];
  const int lane = threadIdx.x & 63, w = threadIdx.x >> 6, fr = lane & 15, fq = lane >> 4;
  const int task = blockIdx.x * 4 + w, seg = task >> 5, h = task & 31;
  u16* Ps = Ps_all[w]; u16* Vts = Vts_all[w];
  const u16* km = kmall + (size_t)seg * 8 * BHD;
  for (int c = lane; c < 128; c += 64) *(u32x4*)(Vts + c * 72) = *(const u32x4*)(vmall + ((size_t)(seg * 32 + h) * 128 + c) * 8);
  for (int c = lane; c < 32 * 16; c += 64) { const int key = 8 + (c >> 4), d8 = c & 15;
    const u32x4 v = *(const u32x4*)(qkvx + (size_t)(seg * 32 + key - 8) * BN3 + 2 * BHD + h * 128 + d8 * 8);
#pragma unroll
    for (int i = 0; i < 4; ++i) { Vts[(d8 * 8 + 2 * i) * 72 + key] = (u16)(v[i] & 0xffff); Vts[(d8 * 8 + 2 * i + 1) * 72 + key] = (u16)(v[i] >> 16); } }
  for (int c = lane; c < 128 * 24; c += 64) Vts[(c / 24) * 72 + 40 + (c % 24)] = 0;
#pragma unroll
  for (int mt = 0; mt < 2; ++mt) {
    f32x4 s[3];
#pragma unroll
    for (int nt = 0; nt < 3; ++nt) s[nt] = f32x4{0.f, 0.f, 0.f, 0.f};
    for (int ks = 0; ks < 4; ++ks) {
      const bf16x8 a = *(const bf16x8*)(qkvx + (size_t)(seg * 32 + mt * 16 + fr) * BN3 + h * 128 + ks * 32 + fq * 8);
#pragma unroll
      for (int nt = 0; nt < 3; ++nt) {
        const int key = nt * 16 + fr;
        bf16x8 b = {0, 0, 0, 0, 0, 0, 0, 0};
        if (key < 8) b = *(const bf16x8*)(km + key * BHD + h * 128 + ks * 32 + fq * 8);
        else if (key < 40) b = *(const bf16x8*)(qkvx + (size_t)(seg * 32 + key - 8) * BN3 + BHD + h * 128 + ks * 32 + fq * 8);
        s[nt] = __builtin_amdgcn_mfma_f32_16x16x32_bf16(a, b, s[nt], 0, 0, 0);
      }
    }
#pragma unroll
    for (int r = 0; r < 4; ++r) {
      const int row = BM_ + mt * 16 + fq * 4 + r;
      float v[3]; float mx = -INFINITY;
#pragma unroll
      for (int nt = 0; nt < 3; ++nt) { const int key = nt * 16 + fr;
        v[nt] = rbf(rbf(s[nt][r]) * BSCALE); if (key > row) v[nt] = rbf(NEGBIG); if (key >= 40) v[nt] = -INFINITY; mx = fmaxf(mx, v[nt]); }
      mx = red16_max(mx);
      float sum = 0.f;
#pragma unroll
      for (int nt = 0; nt < 3; ++nt) { v[nt] = __expf(v[nt] - mx); sum += v[nt]; }
      sum = red16_sum(sum);
      const float inv = 1.0f / sum;
#pragma unroll
      for (int nt = 0; nt < 3; ++nt) Ps[(mt * 16 + fq * 4 + r) * 72 + nt * 16 + fr] = f2bf(v[nt] * inv);
      Ps[(mt * 16 + fq * 4 + r) * 72 + 48 + fr] = 0;
    }
  }
  __syncthreads();
#pragma unroll
  for (int mt = 0; mt < 2; ++mt)
#pragma unroll
    for (int dt = 0; dt < 8; ++dt) {
      f32x4 oa = {0.f, 0.f, 0.f, 0.f};
#pragma unroll
      for (int ks = 0; ks < 2; ++ks) {
        const bf16x8 a = *(const bf16x8*)(Ps + (mt * 16 + fr) * 72 + ks * 32 + fq * 8);
        const bf16x8 b = *(const bf16x8*)(Vts + (dt * 16 + fr) * 72 + ks * 32 + fq * 8);
        oa = __builtin_amdgcn_mfma_f32_16x16x32_bf16(a, b, oa, 0, 0, 0);
      }
#pragma unroll
      for (int r = 0; r < 4; ++r) ox[(size_t)(seg * 32 + mt * 16 + fq * 4 + r) * BHD + h * 128 + dt * 16 + fr] = f2bf(oa[r]);
    }
}

extern "C" void kernel_launch(void* const* d_in, const int* in_sizes, int n_in,
                              void* d_out, int out_size, void* d_ws, size_t ws_size,
                              hipStream_t stream) {
  const u16* A_x = (const u16*)d_in[0];
  const u16* A_wq = (const u16*)d_in[1];
  const u16* A_wk = (const u16*)d_in[2];
  const u16* A_wv = (const u16*)d_in[3];
  const u16* A_wo = (const u16*)d_in[4];
  const u16* B_mem0 = (const u16*)d_in[6];
  const u16* B_Wq = (const u16*)d_in[7];
  const u16* B_Wk = (const u16*)d_in[8];
  const u16* B_Wv = (const u16*)d_in[9];
  const u16* B_Wo = (const u16*)d_in[10];
  u16* out = (u16*)d_out;

  char* ws = (char*)d_ws; size_t off = 0;
  auto alloc = [&](size_t bytes) { char* p = ws + off; off += (bytes + 255) & ~(size_t)255; return (u16*)p; };
  u16* xn = alloc((size_t)S_ * D_ * 2);
  u16* x1 = alloc((size_t)S_ * D_ * 2);
  u16* x2 = alloc((size_t)S_ * D_ * 2);
  u16* x3 = alloc((size_t)S_ * D_ * 2);
  u16* aqkv = alloc((size_t)S_ * AQ_LD * 2);
  u16* ao = alloc((size_t)S_ * D_ * 2);
  u16* Awqkv_t = alloc((size_t)AQ_LD * D_ * 2);
  u16* Awo_t = alloc((size_t)D_ * D_ * 2);
  u16* BWqkv_t = alloc((size_t)BN3 * D_ * 2);
  u16* BWo_t = alloc((size_t)D_ * BHD * 2);
  u16* qkvx = alloc((size_t)S_ * BN3 * 2);
  u16* ox = alloc((size_t)S_ * BHD * 2);
  u16* kmall = alloc((size_t)NSEG * 8 * BHD * 2);
  u16* vmall = alloc((size_t)NSEG * 8 * BHD * 2);
  char* exch0 = ws + off;
  unsigned* tmo = (unsigned*)alloc(256);
  unsigned* memx = (unsigned*)alloc((size_t)2 * (NSEG + 1) * MEMX_SLOT_DW * 4);
  unsigned* inbox = (unsigned*)alloc((size_t)2 * INBOX_DW * 4);
  hipMemsetAsync(exch0, 0xFF, (size_t)((ws + off) - exch0), stream);
  int blayer = 0;

  transpose_kernel<<<dim3(8, 8), 256, 0, stream>>>(A_wq, Awqkv_t, 512, 512);
  transpose_kernel<<<dim3(1, 8), 256, 0, stream>>>(A_wk, Awqkv_t + 512 * 512, 512, 16);
  transpose_kernel<<<dim3(1, 8), 256, 0, stream>>>(A_wv, Awqkv_t + 528 * 512, 512, 16);
  transpose_kernel<<<dim3(8, 8), 256, 0, stream>>>(A_wo, Awo_t, 512, 512);
  transpose_kernel<<<dim3(64, 8), 256, 0, stream>>>(B_Wq, BWqkv_t, 512, 4096);
  transpose_kernel<<<dim3(64, 8), 256, 0, stream>>>(B_Wk, BWqkv_t + (size_t)4096 * 512, 512, 4096);
  transpose_kernel<<<dim3(64, 8), 256, 0, stream>>>(B_Wv, BWqkv_t + (size_t)8192 * 512, 512, 4096);
  transpose_kernel<<<dim3(8, 64), 256, 0, stream>>>(B_Wo, BWo_t, 4096, 512);

  auto layerA = [&](const u16* xin, u16* xout) {
    ln_kernel<<<512, 256, 0, stream>>>(xin, xn);
    gemm_nt<64, 64, 0><<<dim3(9, 32), 256, 0, stream>>>(xn, 512, Awqkv_t, 512, aqkv, AQ_LD, AQ_LD, 512, nullptr);
    attnA_kernel<<<256, 512, 0, stream>>>(aqkv, ao);
    gemm_nt<64, 64, 1><<<dim3(8, 32), 256, 0, stream>>>(ao, 512, Awo_t, 512, xout, 512, 512, 512, xin);
  };
  auto layerB = [&](const u16* xin, u16* xout) {
    ln_kernel<<<512, 256, 0, stream>>>(xin, xn);
    gemm_nt<128, 128, 0><<<dim3(96, 16), 256, 0, stream>>>(xn, 512, BWqkv_t, 512, qkvx, BN3, BN3, 512, nullptr);
    chain_kernel<<<32, 256, 0, stream>>>(BWqkv_t, BWo_t, B_mem0, qkvx, kmall, vmall, memx + (size_t)blayer * (NSEG + 1) * MEMX_SLOT_DW, inbox, tmo);
    ++blayer;
    attnB_kernel<<<512, 256, 0, stream>>>(qkvx, kmall, vmall, ox);
    gemm_nt<64, 64, 1><<<dim3(8, 32), 256, 0, stream>>>(ox, BHD, BWo_t, BHD, xout, 512, 512, BHD, xin);
  };
  layerA(A_x, x1);
  layerB(x1, x2);
  layerA(x2, x3);
  layerB(x3, out);
}
```

```cpp
#include <hip/hip_runtime.h>
#include <hip/hip_bf16.h>
#include <stdint.h>

typedef unsigned short u16;
using bf16x8 = __attribute__((ext_vector_type(8))) short;
using f32x4  = __attribute__((ext_vector_type(4))) float;
using f32x16 = __attribute__((ext_vector_type(16))) float;
using u32x4  = __attribute__((ext_vector_type(4))) unsigned;
using u32x2  = __attribute__((ext_vector_type(2))) unsigned;
#define DI __device__ __forceinline__

DI float bf2f(u16 x) { return __uint_as_float(((unsigned)x) << 16); }
DI u16 f2bf(float f) { __bf16 b = (__bf16)f; return __builtin_bit_cast(u16, b); }
DI float rbf(float f) { return bf2f(f2bf(f)); }
typedef __attribute__((ext_vector_type(2))) __bf16 bf16x2_t;
typedef __attribute__((ext_vector_type(2))) float f32x2_t;
DI unsigned cvtpk(float lo, float hi) { const f32x2_t v = {lo, hi}; return __builtin_bit_cast(unsigned, __builtin_convertvector(v, bf16x2_t)); }
DI unsigned pack2(float lo, float hi) { return cvtpk(lo, hi); }
DI float max3f(float a, float b, float c) { float r; asm("v_max3_f32 %0, %1, %2, %3" : "=v"(r) : "v"(a), "v"(b), "v"(c)); return r; }
DI float max3f_pad(float a, float b, float c) { float r; asm("v_max3_f32 %0, %1, %2, %3\n\ts_nop 1" : "=v"(r) : "v"(a), "v"(b), "v"(c)); return r; }
DI float xmax16(float x) { x = max3f_pad(x, x, x); auto r = __builtin_amdgcn_permlane16_swap(__float_as_uint(x), __float_as_uint(x), false, false); return max3f(__uint_as_float(r[0]), __uint_as_float(r[1]), __uint_as_float(r[1])); }
DI float xmax32(float x) { x = max3f_pad(x, x, x); auto r = __builtin_amdgcn_permlane32_swap(__float_as_uint(x), __float_as_uint(x), false, false); return max3f(__uint_as_float(r[0]), __uint_as_float(r[1]), __uint_as_float(r[1])); }
DI float xsum16(float x) { auto r = __builtin_amdgcn_permlane16_swap(__float_as_uint(x), __float_as_uint(x), false, false); return __uint_as_float(r[0]) + __uint_as_float(r[1]); }
DI float xsum32(float x) { auto r = __builtin_amdgcn_permlane32_swap(__float_as_uint(x), __float_as_uint(x), false, false); return __uint_as_float(r[0]) + __uint_as_float(r[1]); }

constexpr int S_ = 2048, D_ = 512;
constexpr int AH = 32, ADH = 16;
constexpr int BH = 32, BDH = 128, BM_ = 8, SEG = 32, NSEG = 64;
constexpr int BN3 = 3 * BH * BDH;
constexpr int BHD = BH * BDH;

__global__ __launch_bounds__(256) void ln_kernel(const u16* __restrict__ x, u16* __restrict__ y) {
  const int row = blockIdx.x * 4 + (threadIdx.x >> 6), lane = threadIdx.x & 63;
  const u32x4 v = *(const u32x4*)(x + (size_t)row * D_ + lane * 8);
  float f[8];
#pragma unroll
  for (int i = 0; i < 4; ++i) { f[2 * i] = __uint_as_float(v[i] << 16); f[2 * i + 1] = __uint_as_float(v[i] & 0xffff0000u); }
  float s = 0.f;
#pragma unroll
  for (int i = 0; i < 8; ++i) s += f[i];
#pragma unroll
  for (int o = 32; o >= 1; o >>= 1) s += __shfl_xor(s, o);
  const float mean = s * (1.0f / D_);
  float q = 0.f;
#pragma unroll
  for (int i = 0; i < 8; ++i) { const float d = f[i] - mean; q += d * d; }
#pragma unroll
  for (int o = 32; o >= 1; o >>= 1) q += __shfl_xor(q, o);
  const float var = q * (1.0f / D_);
  const float mb = rbf(mean), vb = rbf(var);
  const float den = rbf(sqrtf(rbf(vb + 1e-5f)));
  u32x4 o;
#pragma unroll
  for (int i = 0; i < 4; ++i) o[i] = pack2(rbf(f[2 * i] - mb) / den, rbf(f[2 * i + 1] - mb) / den);
  *(u32x4*)(y + (size_t)row * D_ + lane * 8) = o;
}

struct TrJobs { const u16* src[8]; u16* dst[8]; int K[8]; int N[8]; int tile0[9]; int pad_; };
__global__ __launch_bounds__(256) void transpose_all_kernel(TrJobs jobs) {
  __shared__ __attribute__((aligned(16))) u16 t[64][68];
  int j = 0;
#pragma unroll
  for (int i = 1; i < 8; ++i) j += (int)blockIdx.x >= jobs.tile0[i];
  const int K = jobs.K[j], N = jobs.N[j], tl = blockIdx.x - jobs.tile0[j];
  const int ntn = (N + 63) >> 6, n0 = (tl % ntn) * 64, k0 = (tl / ntn) * 64;
  const u16* __restrict__ in = jobs.src[j]; u16* __restrict__ out = jobs.dst[j];
  const int tx = threadIdx.x & 15, ty = threadIdx.x >> 4;
#pragma unroll
  for (int i = 0; i < 4; ++i) {
    const int r = ty + 16 * i, c = tx * 4;
    if (n0 + c < N) {
      const u32x2 v = *(const u32x2*)(in + (size_t)(k0 + r) * N + n0 + c);
      t[c][r] = (u16)(v[0] & 0xffff); t[c + 1][r] = (u16)(v[0] >> 16); t[c + 2][r] = (u16)(v[1] & 0xffff); t[c + 3][r] = (u16)(v[1] >> 16);
    }
  }
  __syncthreads();
#pragma unroll
  for (int i = 0; i < 4; ++i) {
    const int n = ty + 16 * i;
    if (n0 + n < N) *(u32x2*)(out + (size_t)(n0 + n) * K + k0 + tx * 4) = *(const u32x2*)(&t[n][tx * 4]);
  }
}

template <int BM, int BN, int EPI>
__global__ __launch_bounds__(256) void gemm_nt(const u16* __restrict__ A, int lda, const u16* __restrict__ Bt, int ldb,
                                               u16* __restrict__ C, int ldc, int N, int K, const u16* __restrict__ R, u16* __restrict__ X) {
  constexpr int BK = 64, LS = 72;
  constexpr int MT = BM / 32, NT = BN / 32;
  constexpr int CA = BM * 8 / 256, CB = BN * 8 / 256;
  constexpr int LDS_AB = (BM + BN) * LS * 2, LDS_C = BM * (BN + 8) * 2;
  __shared__ __attribute__((aligned(16))) char smem[LDS_AB > LDS_C ? LDS_AB : LDS_C];
  u16* As = (u16*)smem; u16* Bs = As + BM * LS;
  const int tid = threadIdx.x, lane = tid & 63, w = tid >> 6, wm = w >> 1, wn = w & 1;
  const int m0 = blockIdx.y * BM, n0 = blockIdx.x * BN;
  const int fr = lane & 15, fq = lane >> 4;
  f32x4 acc[MT][NT];
#pragma unroll
  for (int i = 0; i < MT; ++i)
#pragma unroll
    for (int j = 0; j < NT; ++j) acc[i][j] = f32x4{0.f, 0.f, 0.f, 0.f};
  u32x4 ra[CA], rb[CB];
  auto gload = [&](int k0) {
#pragma unroll
    for (int i = 0; i < CA; ++i) { const int c = tid + i * 256, r = c >> 3, c8 = c & 7; ra[i] = *(const u32x4*)(A + (size_t)(m0 + r) * lda + k0 + c8 * 8); }
#pragma unroll
    for (int i = 0; i < CB; ++i) { const int c = tid + i * 256, r = c >> 3, c8 = c & 7; int n = n0 + r; n = n < N ? n : N - 1;
      rb[i] = *(const u32x4*)(Bt + (size_t)n * ldb + k0 + c8 * 8); }
  };
  gload(0);
  for (int k0 = 0; k0 < K; k0 += BK) {
    __syncthreads();
#pragma unroll
    for (int i = 0; i < CA; ++i) { const int c = tid + i * 256, r = c >> 3, c8 = c & 7; *(u32x4*)(As + r * LS + c8 * 8) = ra[i]; }
#pragma unroll
    for (int i = 0; i < CB; ++i) { const int c = tid + i * 256, r = c >> 3, c8 = c & 7; *(u32x4*)(Bs + r * LS + c8 * 8) = rb[i]; }
    __syncthreads();
    if (k0 + BK < K) gload(k0 + BK);
#pragma unroll
    for (int kk = 0; kk < 2; ++kk) {
      bf16x8 af[MT], bfr[NT];
#pragma unroll
      for (int i = 0; i < MT; ++i) af[i] = *(const bf16x8*)(As + (wm * (BM / 2) + i * 16 + fr) * LS + kk * 32 + fq * 8);
#pragma unroll
      for (int j = 0; j < NT; ++j) bfr[j] = *(const bf16x8*)(Bs + (wn * (BN / 2) + j * 16 + fr) * LS + kk * 32 + fq * 8);
#pragma unroll
      for (int i = 0; i < MT; ++i)
#pragma unroll
        for (int j = 0; j < NT; ++j) acc[i][j] = __builtin_amdgcn_mfma_f32_16x16x32_bf16(af[i], bfr[j], acc[i][j], 0, 0, 0);
    }
  }
  if ((EPI == 2 && n0 >= 2 * BHD) || (EPI == 3 && n0 + BN > 512)) {
#pragma unroll
    for (int i = 0; i < MT; ++i)
#pragma unroll
      for (int j = 0; j < NT; ++j) {
        const int n = n0 + wn * (BN / 2) + j * 16 + fr, m = m0 + wm * (BM / 2) + i * 16 + fq * 4;
        u32x2 pk; pk[0] = pack2(acc[i][j][0], acc[i][j][1]); pk[1] = pack2(acc[i][j][2], acc[i][j][3]);
        if (EPI == 2) {
          const int hh = (n - 2 * BHD) >> 7, dim = n & 127;
          *(u32x2*)(X + ((size_t)((m >> 5) * 32 + hh) * 128 + dim) * 32 + (m & 31)) = pk;
        } else if (n >= 512 && n < 528) {
#pragma unroll
          for (int r = 0; r < 4; ++r) X[(size_t)(m + r) * 16 + (n - 512)] = (u16)(r & 1 ? pk[r >> 1] >> 16 : pk[r >> 1] & 0xffff);
        } else if (n >= 528 && n < 544) {
          const int pos = (m & ~15) | ((fq & 1) * 8 + (fq >> 1) * 4);
          *(u32x2*)(X + 2048 * 16 + (size_t)(n - 528) * 2048 + pos) = pk;
        } else if (n < 512) {
#pragma unroll
          for (int r = 0; r < 4; ++r) C[(size_t)(m + r) * ldc + n] = (u16)(r & 1 ? pk[r >> 1] >> 16 : pk[r >> 1] & 0xffff);
        }
      }
    return;
  }
  __syncthreads();
  constexpr int CS = BN + 8;
  u16* Cs = (u16*)smem;
#pragma unroll
  for (int i = 0; i < MT; ++i)
#pragma unroll
    for (int j = 0; j < NT; ++j)
#pragma unroll
      for (int r = 0; r < 4; ++r) Cs[(wm * (BM / 2) + i * 16 + fq * 4 + r) * CS + wn * (BN / 2) + j * 16 + fr] = f2bf(acc[i][j][r]);
  __syncthreads();
  for (int c = tid; c < BM * BN / 8; c += 256) {
    const int r = c / (BN / 8), c8 = c % (BN / 8);
    const int n = n0 + c8 * 8;
    if (n >= N) continue;
    u32x4 v = *(const u32x4*)(Cs + r * CS + c8 * 8);
    if (EPI == 1) {
      const u32x4 rr = *(const u32x4*)(R + (size_t)(m0 + r) * ldc + n);
#pragma unroll
      for (int i = 0; i < 4; ++i) {
        const float lo = bf2f((u16)(v[i] & 0xffff)) + bf2f((u16)(rr[i] & 0xffff));
        const float hi = bf2f((u16)(v[i] >> 16)) + bf2f((u16)(rr[i] >> 16));
        v[i] = pack2(lo, hi);
      }
    }
    *(u32x4*)(C + (size_t)(m0 + r) * ldc + n) = v;
  }
}

constexpr int AQ_LD = 544;
constexpr int VT_LD = 2048 + 8;
__global__ __launch_bounds__(512) void attnA_kernel(const u16* __restrict__ qkv, const u16* __restrict__ kvx, u16* __restrict__ o) {
  __shared__ __attribute__((aligned(16))) u16 Ks[2048 * 16];
  __shared__ __attribute__((aligned(16))) u16 Vt[16 * VT_LD];
  const int tid = threadIdx.x, lane = tid & 63, w = tid >> 6;
  for (int c = tid; c < 2048 * 2; c += 512) *(u32x4*)(Ks + c * 8) = *(const u32x4*)(kvx + c * 8);
  for (int c = tid; c < 16 * 256; c += 512) { const int d = c >> 8, k8 = c & 255; *(u32x4*)(Vt + d * VT_LD + k8 * 8) = *(const u32x4*)(kvx + 2048 * 16 + d * 2048 + k8 * 8); }
  __syncthreads();
  const int tok = blockIdx.x * 8 + w;
  const int c32 = lane & 31, hh = lane >> 5;
  const bf16x8 qb = *(const bf16x8*)(qkv + (size_t)tok * AQ_LD + c32 * 16 + hh * 8);
  const float cs = 0.25f * 1.4426950408889634f;
  float m_run = -INFINITY, l_run = 0.f;
  f32x16 oacc;
#pragma unroll
  for (int i = 0; i < 16; ++i) oacc[i] = 0.f;
  const f32x16 zero16 = oacc;
  const u16* kp = Ks + c32 * 16 + hh * 8;
  const u16* vp = Vt + (lane & 15) * VT_LD + hh * 8;
  for (int t = 0; t < 64; ++t) {
    const bf16x8 ka = *(const bf16x8*)(kp + t * 32 * 16);
    f32x16 x = __builtin_amdgcn_mfma_f32_32x32x16_bf16(ka, qb, zero16, 0, 0, 0);
#pragma unroll
    for (int i = 0; i < 16; i += 2) { const unsigned c = cvtpk(x[i], x[i + 1]); x[i] = __uint_as_float(c << 16); x[i + 1] = __uint_as_float(c & 0xffff0000u); }
    float mt = max3f(x[0], x[1], x[2]);
#pragma unroll
    for (int i = 3; i < 15; i += 2) mt = max3f(mt, x[i], x[i + 1]);
    mt = max3f(mt, x[15], x[15]);
    mt = xmax32(mt);
    const float m_new = max3f(m_run, mt, mt);
    const float alpha = __builtin_amdgcn_exp2f((m_run - m_new) * cs);
    const float mc = m_new * cs;
    float ls = 0.f;
    u32x4 pk0, pk1;
#pragma unroll
    for (int i = 0; i < 16; i += 2) {
      const float e0 = __builtin_amdgcn_exp2f(__builtin_fmaf(x[i], cs, -mc)), e1 = __builtin_amdgcn_exp2f(__builtin_fmaf(x[i + 1], cs, -mc));
      ls += e0 + e1;
      const unsigned pk = cvtpk(e0, e1);
      if (i < 8) pk0[i >> 1] = pk; else pk1[(i - 8) >> 1] = pk;
    }
    l_run = __builtin_fmaf(l_run, alpha, ls);
    m_run = m_new;
#pragma unroll
    for (int i = 0; i < 8; ++i) oacc[i] *= alpha;
    {
      const bf16x8 va0 = *(const bf16x8*)(vp + t * 32), va1 = *(const bf16x8*)(vp + t * 32 + 16);
      oacc = __builtin_amdgcn_mfma_f32_32x32x16_bf16(va0, __builtin_bit_cast(bf16x8, pk0), oacc, 0, 0, 0);
      oacc = __builtin_amdgcn_mfma_f32_32x32x16_bf16(va1, __builtin_bit_cast(bf16x8, pk1), oacc, 0, 0, 0);
    }
  }
  l_run += __shfl_xor(l_run, 32);
  const float inv = 1.0f / l_run;
  u32x2 o0, o1;
  o0[0] = pack2(oacc[0] * inv, oacc[1] * inv); o0[1] = pack2(oacc[2] * inv, oacc[3] * inv);
  o1[0] = pack2(oacc[4] * inv, oacc[5] * inv); o1[1] = pack2(oacc[6] * inv, oacc[7] * inv);
  u16* op = o + (size_t)tok * 512 + c32 * 16;
  *(u32x2*)(op + 4 * hh) = o0;
  *(u32x2*)(op + 8 + 4 * hh) = o1;
}

DI float red16_max(float v) { v = fmaxf(v, __shfl_xor(v, 1)); v = fmaxf(v, __shfl_xor(v, 2)); v = fmaxf(v, __shfl_xor(v, 4)); v = fmaxf(v, __shfl_xor(v, 8)); return v; }
DI float red16_sum(float v) { v += __shfl_xor(v, 1); v += __shfl_xor(v, 2); v += __shfl_xor(v, 4); v += __shfl_xor(v, 8); return v; }

constexpr float BSCALE = 0.08838834764831845f;
constexpr float NEGBIG = -1000000000.0f;

__global__ __launch_bounds__(256) void mem_proj_kernel(const u16* __restrict__ mem, const u16* __restrict__ Wt,
                                                       u16* __restrict__ qm, u16* __restrict__ km, u16* __restrict__ vm) {
  const int lane = threadIdx.x & 63, w = threadIdx.x >> 6, fr = lane & 15, fq = lane >> 4;
  const int n0 = (blockIdx.x * 4 + w) * 16;
  f32x4 acc = {0.f, 0.f, 0.f, 0.f};
  for (int ks = 0; ks < 16; ++ks) {
    bf16x8 a = {0, 0, 0, 0, 0, 0, 0, 0};
    if (fr < 8) a = *(const bf16x8*)(mem + fr * 512 + ks * 32 + fq * 8);
    const bf16x8 b = *(const bf16x8*)(Wt + (size_t)(n0 + fr) * 512 + ks * 32 + fq * 8);
    acc = __builtin_amdgcn_mfma_f32_16x16x32_bf16(a, b, acc, 0, 0, 0);
  }
  if (fq < 2) {
    const int which = n0 / BHD, n = n0 % BHD + fr;
    u16* dst = which == 0 ? qm : (which == 1 ? km : vm);
#pragma unroll
    for (int r = 0; r < 4; ++r) dst[(fq * 4 + r) * BHD + n] = f2bf(acc[r]);
  }
}

__global__ __launch_bounds__(64) void mem_attn_kernel(const u16* __restrict__ qm, const u16* __restrict__ km, const u16* __restrict__ vm,
                                                      const u16* __restrict__ qkvx, int seg, u16* __restrict__ om) {
  __shared__ __attribute__((aligned(16))) u16 Ps[16 * 72];
  __shared__ __attribute__((aligned(16))) u16 Vts[128 * 72];
  const int lane = threadIdx.x, fr = lane & 15, fq = lane >> 4, h = blockIdx.x;
  for (int c = lane; c < 40 * 16; c += 64) { const int key = c >> 4, d8 = c & 15;
    const u16* src = key < 8 ? vm + key * BHD + h * 128 + d8 * 8 : qkvx + (size_t)(seg * 32 + key - 8) * BN3 + 2 * BHD + h * 128 + d8 * 8;
    const u32x4 v = *(const u32x4*)src;
#pragma unroll
    for (int i = 0; i < 4; ++i) { Vts[(d8 * 8 + 2 * i) * 72 + key] = (u16)(v[i] & 0xffff); Vts[(d8 * 8 + 2 * i + 1) * 72 + key] = (u16)(v[i] >> 16); } }
  for (int c = lane; c < 128 * 24; c += 64) Vts[(c / 24) * 72 + 40 + (c % 24)] = 0;
  f32x4 s[3];
#pragma unroll
  for (int nt = 0; nt < 3; ++nt) s[nt] = f32x4{0.f, 0.f, 0.f, 0.f};
  for (int ks = 0; ks < 4; ++ks) {
    bf16x8 a = {0, 0, 0, 0, 0, 0, 0, 0};
    if (fr < 8) a = *(const bf16x8*)(qm + fr * BHD + h * 128 + ks * 32 + fq * 8);
#pragma unroll
    for (int nt = 0; nt < 3; ++nt) {
      const int key = nt * 16 + fr;
      bf16x8 b = {0, 0, 0, 0, 0, 0, 0, 0};
      if (key < 8) b = *(const bf16x8*)(km + key * BHD + h * 128 + ks * 32 + fq * 8);
      else if (key < 40) b = *(const bf16x8*)(qkvx + (size_t)(seg * 32 + key - 8) * BN3 + BHD + h * 128 + ks * 32 + fq * 8);
      s[nt] = __builtin_amdgcn_mfma_f32_16x16x32_bf16(a, b, s[nt], 0, 0, 0);
    }
  }
#pragma unroll
  for (int r = 0; r < 4; ++r) {
    float v[3]; float mx = -INFINITY;
#pragma unroll
    for (int nt = 0; nt < 3; ++nt) { v[nt] = rbf(rbf(s[nt][r]) * BSCALE); if (nt * 16 + fr >= 40) v[nt] = -INFINITY; mx = fmaxf(mx, v[nt]); }
    mx = red16_max(mx);
    float sum = 0.f;
#pragma unroll
    for (int nt = 0; nt < 3; ++nt) { v[nt] = __expf(v[nt] - mx); sum += v[nt]; }
    sum = red16_sum(sum);
    const float inv = 1.0f / sum;
#pragma unroll
    for (int nt = 0; nt < 3; ++nt) Ps[(fq * 4 + r) * 72 + nt * 16 + fr] = f2bf(v[nt] * inv);
    Ps[(fq * 4 + r) * 72 + 48 + fr] = 0;
  }
  __syncthreads();
#pragma unroll
  for (int dt = 0; dt < 8; ++dt) {
    f32x4 oa = {0.f, 0.f, 0.f, 0.f};
#pragma unroll
    for (int ks = 0; ks < 2; ++ks) {
      const bf16x8 a = *(const bf16x8*)(Ps + fr * 72 + ks * 32 + fq * 8);
      const bf16x8 b = *(const bf16x8*)(Vts + (dt * 16 + fr) * 72 + ks * 32 + fq * 8);
      oa = __builtin_amdgcn_mfma_f32_16x16x32_bf16(a, b, oa, 0, 0, 0);
    }
    if (fq < 2) {
#pragma unroll
      for (int r = 0; r < 4; ++r) om[(fq * 4 + r) * BHD + h * 128 + dt * 16 + fr] = f2bf(oa[r]);
    }
  }
}

__global__ __launch_bounds__(256) void mem_out_kernel(const u16* __restrict__ om, const u16* __restrict__ Wot, u16* __restrict__ mem_out) {
  __shared__ float red[4][16][16];
  const int lane = threadIdx.x & 63, w = threadIdx.x >> 6, fr = lane & 15, fq = lane >> 4;
  const int n0 = blockIdx.x * 16;
  f32x4 acc = {0.f, 0.f, 0.f, 0.f};
  for (int ks = w * 32; ks < w * 32 + 32; ++ks) {
    bf16x8 a = {0, 0, 0, 0, 0, 0, 0, 0};
    if (fr < 8) a = *(const bf16x8*)(om + fr * BHD + ks * 32 + fq * 8);
    const bf16x8 b = *(const bf16x8*)(Wot + (size_t)(n0 + fr) * BHD + ks * 32 + fq * 8);
    acc = __builtin_amdgcn_mfma_f32_16x16x32_bf16(a, b, acc, 0, 0, 0);
  }
#pragma unroll
  for (int r = 0; r < 4; ++r) red[w][fq * 4 + r][fr] = acc[r];
  __syncthreads();
  if (threadIdx.x < 128) {
    const int r = threadIdx.x >> 4, c = threadIdx.x & 15;
    mem_out[r * 512 + n0 + c] = f2bf(red[0][r][c] + red[1][r][c] + red[2][r][c] + red[3][r][c]);
  }
}

constexpr unsigned SENT = 0xFFFFFFFFu;
#define AUX_SC1 16
constexpr int KS_LD = 136, VT_LDB = 40, OS_LD = 72;
constexpr int NCH = 64;
constexpr int L_WV = 0, L_WV_BYTES = 4 * 16 * 1024;
constexpr int L_WO = L_WV + L_WV_BYTES, L_WO_BYTES = 32 * 2 * 1024;
constexpr int L_VT = L_WO + L_WO_BYTES, L_VT_BYTES = 64 * VT_LDB * 2;
constexpr int L_K = L_VT + L_VT_BYTES, L_K_BYTES = 40 * KS_LD * 2;
constexpr int L_Q = L_K + L_K_BYTES, L_Q_BYTES = 8 * KS_LD * 2;
constexpr int L_MX = L_Q + L_Q_BYTES, L_MX_BYTES = 8192;
constexpr int L_O = L_MX;
constexpr int L_RS = L_MX + L_MX_BYTES;
constexpr int L_TOTAL = L_RS + 2048;
constexpr int MEMX_SLOT_DW = 64 * 32;
constexpr int INBOX_DW = 64 * 64 * 64;
constexpr unsigned SPIN_LIMIT = 1u << 15;
#define CH_DELAY1 0
#define CH_DELAY2 4

#define STAMP(i)
DI bool valid4(u32x4 v) { return v[0] != SENT && v[1] != SENT && v[2] != SENT && v[3] != SENT; }
DI unsigned canon(unsigned x) { return x == SENT ? 0x7FC07FC0u : x; }

#define LBAR() asm volatile("s_waitcnt lgkmcnt(0)\n\ts_barrier" ::: "memory")
constexpr int CT = 512;
__global__ __launch_bounds__(CT, 2) void chain_kernel(const u16* __restrict__ Wqkv_t, const u16* __restrict__ Wo_t, const u16* __restrict__ mem0,
                                                      const u16* __restrict__ qkvx, const u16* __restrict__ vtx, u16* __restrict__ kmall, u16* __restrict__ vmTall,
                                                      unsigned* memx, unsigned* inbox, unsigned* tmo) {
  __shared__ __attribute__((aligned(16))) char smem[L_TOTAL];
  const int b = blockIdx.x, h = b >> 1, a = b & 1;
  const int tid = threadIdx.x, lane = tid & 63, w = __builtin_amdgcn_readfirstlane(tid >> 6), fr = lane & 15, g = lane >> 4;
  u16* VT = (u16*)(smem + L_VT); u16* KALL = (u16*)(smem + L_K); u16* QS = (u16*)(smem + L_Q); u16* OS = (u16*)(smem + L_O);
  const __amdgpu_buffer_rsrc_t rmx = __builtin_amdgcn_make_buffer_rsrc((void*)memx, 0, (NSEG + 1) * MEMX_SLOT_DW * 4, 0x00020000);
  const __amdgpu_buffer_rsrc_t rin = __builtin_amdgcn_make_buffer_rsrc((void*)inbox, 0, 2 * INBOX_DW * 4, 0x00020000);
  const __amdgpu_buffer_rsrc_t rtm = __builtin_amdgcn_make_buffer_rsrc((void*)tmo, 0, 64, 0x00020000);
  const __amdgpu_buffer_rsrc_t rqk = __builtin_amdgcn_make_buffer_rsrc((void*)qkvx, 0, S_ * BN3 * 2, 0x00020000);
  const __amdgpu_buffer_rsrc_t rvx = __builtin_amdgcn_make_buffer_rsrc((void*)vtx, 0, S_ * BHD * 2, 0x00020000);
  const __amdgpu_buffer_rsrc_t rkm = __builtin_amdgcn_make_buffer_rsrc((void*)kmall, 0, NSEG * 8 * BHD * 2, 0x00020000);
  const __amdgpu_buffer_rsrc_t rvm = __builtin_amdgcn_make_buffer_rsrc((void*)vmTall, 0, NSEG * 8 * BHD * 2, 0x00020000);

  bf16x8 wf[2][16];
#pragma unroll
  for (int t = 0; t < 2; ++t) {
    const u16* src = Wqkv_t + (size_t)(t * BHD + h * 128 + 16 * w + fr) * 512 + g * 8;
#pragma unroll
    for (int ks = 0; ks < 16; ++ks) wf[t][ks] = *(const bf16x8*)(src + ks * 32);
  }
  {
#pragma unroll
    for (int i = 0; i < 8; ++i) { const int f = w * 8 + i, j = f >> 4, ks = f & 15;
      *(bf16x8*)(smem + L_WV + (f * 64 + lane) * 16) = *(const bf16x8*)(Wqkv_t + (size_t)(2 * BHD + h * 128 + 64 * a + 16 * j + fr) * 512 + ks * 32 + g * 8); }
#pragma unroll
    for (int i = 0; i < 8; ++i) { const int f = w * 8 + i, nt = f >> 1, ks2 = f & 1;
      *(bf16x8*)(smem + L_WO + (f * 64 + lane) * 16) = *(const bf16x8*)(Wo_t + (size_t)(16 * nt + fr) * BHD + h * 128 + 64 * a + ks2 * 32 + g * 8); }
  }
  for (int c = tid; c < L_VT_BYTES / 16; c += CT) *(u32x4*)(smem + L_VT + c * 16) = u32x4{0u, 0u, 0u, 0u};
  LBAR();

  u32x4 kst, vst;
  const int ktok = tid >> 4, kd8 = tid & 15;
  const int vdim = tid >> 2, vc = tid & 3;
  const int k_voff = (ktok * BN3 + BHD + h * 128 + kd8 * 8) * 2;
  const int v_voff = ((h * 128 + 64 * a + vdim) * 32 + vc * 8) * 2;
  auto kv_load = [&](int s) {
    kst = __builtin_amdgcn_raw_buffer_load_b128(rqk, k_voff, s * 32 * BN3 * 2, 0);
    if (tid < 256) vst = __builtin_amdgcn_raw_buffer_load_b128(rvx, v_voff, s * 32 * 128 * 32 * 2, 0);
  };
  auto kv_store = [&]() {
    *(u32x4*)(KALL + (8 + ktok) * KS_LD + kd8 * 8) = kst;
    if (tid < 256) *(u32x4*)(VT + vdim * VT_LDB + 8 + vc * 8) = vst;
  };
  const int km_voff = (fr * BHD + h * 128 + 16 * w + 4 * g) * 2;
  const int vm_voff = ((h * 128 + 64 * a + 16 * w + fr) * 8 + 4 * g) * 2;
  const int ib_voff = (((g >> 1) * 64 + b) * 64 + fr * 8 + 4 * (g & 1)) * 4;
  const int ip_voff = (b * 64 * 64 + tid * 4) * 4;
  const int mp_voff = tid * 16;
  kv_load(0);
  kv_store();
  { const int r = tid >> 6, k = (tid & 63) * 8;
    *(u32x4*)(smem + L_MX + (k >> 3) * 128 + r * 16) = *(const u32x4*)(mem0 + r * 512 + k); }

  const char* mxl = smem + L_MX + g * 128 + (fr & 7) * 16;
  for (int s = 0; s < NSEG; ++s) {
    LBAR();
    if (s + 1 < NSEG) kv_load(s + 1);
    f32x4 acc[2];
#pragma unroll
    for (int t = 0; t < 2; ++t) acc[t] = f32x4{0.f, 0.f, 0.f, 0.f};
    {
      bf16x8 mfa = *(const bf16x8*)(mxl), mfb = *(const bf16x8*)(mxl + 512);
#pragma unroll
      for (int ks = 0; ks < 16; ++ks) {
        const bf16x8 mf = (ks & 1) ? mfb : mfa;
        if (ks + 2 < 16) { if (ks & 1) mfb = *(const bf16x8*)(mxl + (ks + 2) * 512); else mfa = *(const bf16x8*)(mxl + (ks + 2) * 512); }
        acc[0] = __builtin_amdgcn_mfma_f32_16x16x32_bf16(wf[0][ks], mf, acc[0], 0, 0, 0);
        acc[1] = __builtin_amdgcn_mfma_f32_16x16x32_bf16(wf[1][ks], mf, acc[1], 0, 0, 0);
      }
    }
    STAMP(0);
    {
      const int n0 = 16 * w + 4 * g;
      u32x2 pq, pk2;
      pq[0] = pack2(acc[0][0], acc[0][1]); pq[1] = pack2(acc[0][2], acc[0][3]);
      pk2[0] = pack2(acc[1][0], acc[1][1]); pk2[1] = pack2(acc[1][2], acc[1][3]);
      if (fr < 8) { *(u32x2*)(QS + fr * KS_LD + n0) = pq; *(u32x2*)(KALL + fr * KS_LD + n0) = pk2;
                    if (a == 0) __builtin_amdgcn_raw_buffer_store_b64(pk2, rkm, km_voff, s * 8 * BHD * 2, 0); }
    }
    LBAR();
    STAMP(1);
    bf16x8 pf[2];
    if (w < 4) {
      const char* wvb = smem + L_WV + (w * 16 * 64 + lane) * 16;
      f32x4 av = {0.f, 0.f, 0.f, 0.f};
      bf16x8 mfa = *(const bf16x8*)(mxl), mfb = *(const bf16x8*)(mxl + 512);
      bf16x8 wva = *(const bf16x8*)(wvb), wvb2 = *(const bf16x8*)(wvb + 1024);
#pragma unroll
      for (int ks = 0; ks < 16; ++ks) {
        const bf16x8 mf = (ks & 1) ? mfb : mfa, wv = (ks & 1) ? wvb2 : wva;
        if (ks + 2 < 16) {
          if (ks & 1) { mfb = *(const bf16x8*)(mxl + (ks + 2) * 512); wvb2 = *(const bf16x8*)(wvb + (ks + 2) * 1024); }
          else { mfa = *(const bf16x8*)(mxl + (ks + 2) * 512); wva = *(const bf16x8*)(wvb + (ks + 2) * 1024); }
        }
        av = __builtin_amdgcn_mfma_f32_16x16x32_bf16(mf, wv, av, 0, 0, 0);
      }
      u32x2 pv2; pv2[0] = pack2(av[0], av[1]); pv2[1] = pack2(av[2], av[3]);
      if (g < 2) { *(u32x2*)(VT + (16 * w + fr) * VT_LDB + 4 * g) = pv2; __builtin_amdgcn_raw_buffer_store_b64(pv2, rvm, vm_voff, s * 32 * 128 * 8 * 2, 0); }
    } else {
      bf16x8 qf[4];
#pragma unroll
      for (int ks = 0; ks < 4; ++ks) qf[ks] = *(const bf16x8*)(QS + (fr & 7) * KS_LD + 32 * ks + 8 * g);
      f32x4 sc[3];
      {
        const u16* kb = KALL + fr * KS_LD + 8 * g;
        bf16x8 kf[3][4];
#pragma unroll
        for (int t = 0; t < 3; ++t)
#pragma unroll
          for (int ks = 0; ks < 4; ++ks) kf[t][ks] = *(const bf16x8*)(kb + 16 * t * KS_LD + 32 * ks);
#pragma unroll
        for (int t = 0; t < 3; ++t) sc[t] = f32x4{0.f, 0.f, 0.f, 0.f};
#pragma unroll
        for (int ks = 0; ks < 4; ++ks)
#pragma unroll
          for (int t = 0; t < 3; ++t) sc[t] = __builtin_amdgcn_mfma_f32_16x16x32_bf16(kf[t][ks], qf[ks], sc[t], 0, 0, 0);
      }
      float pv[3][4]; float mx = -INFINITY;
#pragma unroll
      for (int t = 0; t < 3; ++t)
#pragma unroll
        for (int r = 0; r < 4; r += 2) {
          const unsigned c1 = pack2(sc[t][r], sc[t][r + 1]);
          const unsigned c2 = pack2(__uint_as_float(c1 << 16) * BSCALE, __uint_as_float(c1 & 0xffff0000u) * BSCALE);
          float v0 = __uint_as_float(c2 << 16), v1 = __uint_as_float(c2 & 0xffff0000u);
          if (t == 2 && g >= 2) { v0 = -INFINITY; v1 = -INFINITY; }
          pv[t][r] = v0; pv[t][r + 1] = v1; mx = fmaxf(mx, fmaxf(v0, v1));
        }
      mx = xmax16(mx); mx = xmax32(mx);
      float sum = 0.f;
#pragma unroll
      for (int t = 0; t < 3; ++t)
#pragma unroll
        for (int r = 0; r < 4; ++r) { pv[t][r] = __expf(pv[t][r] - mx); sum += pv[t][r]; }
      sum = xsum16(sum); sum = xsum32(sum);
      const float inv = 1.0f / sum;
      u32x4 p0, p1;
      p0[0] = pack2(pv[0][0] * inv, pv[0][1] * inv); p0[1] = pack2(pv[0][2] * inv, pv[0][3] * inv);
      p0[2] = pack2(pv[1][0] * inv, pv[1][1] * inv); p0[3] = pack2(pv[1][2] * inv, pv[1][3] * inv);
      p1[0] = pack2(pv[2][0] * inv, pv[2][1] * inv); p1[1] = pack2(pv[2][2] * inv, pv[2][3] * inv);
      p1[2] = 0u; p1[3] = 0u;
      pf[0] = __builtin_bit_cast(bf16x8, p0); pf[1] = __builtin_bit_cast(bf16x8, p1);
    }
    LBAR();
    if (w >= 4) {
      const u16* vrow = VT + (16 * (w & 3) + fr) * VT_LDB + 4 * g;
      const u32x2 v00 = *(const u32x2*)(vrow), v01 = *(const u32x2*)(vrow + 16), v10 = *(const u32x2*)(vrow + 32), v11 = *(const u32x2*)(vrow + 48);
      const u32x4 va0 = {v00[0], v00[1], v01[0], v01[1]}, va1 = {v10[0], v10[1], v11[0], v11[1]};
      f32x4 oa = {0.f, 0.f, 0.f, 0.f};
      oa = __builtin_amdgcn_mfma_f32_16x16x32_bf16(__builtin_bit_cast(bf16x8, va0), pf[0], oa, 0, 0, 0);
      oa = __builtin_amdgcn_mfma_f32_16x16x32_bf16(__builtin_bit_cast(bf16x8, va1), pf[1], oa, 0, 0, 0);
      u32x2 pk; pk[0] = pack2(oa[0], oa[1]); pk[1] = pack2(oa[2], oa[3]);
      if (fr < 8) *(u32x2*)(OS + fr * OS_LD + 16 * (w & 3) + 4 * g) = pk;
    }
    LBAR();
    STAMP(2);
    if (s + 1 == NSEG) break;
    const int slot = s & 1;
    {
      bf16x8 of[2];
#pragma unroll
      for (int ks = 0; ks < 2; ++ks) of[ks] = *(const bf16x8*)(OS + (fr & 7) * OS_LD + 32 * ks + 8 * g);
      const char* wob = smem + L_WO + (w * 8 * 64 + lane) * 16;
      bf16x8 wo[8];
#pragma unroll
      for (int i = 0; i < 8; ++i) wo[i] = *(const bf16x8*)(wob + i * 1024);
#pragma unroll
      for (int i = 0; i < 4; ++i) {
        f32x4 acc2 = {0.f, 0.f, 0.f, 0.f};
        acc2 = __builtin_amdgcn_mfma_f32_16x16x32_bf16(wo[2 * i], of[0], acc2, 0, 0, 0);
        acc2 = __builtin_amdgcn_mfma_f32_16x16x32_bf16(wo[2 * i + 1], of[1], acc2, 0, 0, 0);
        u32x4 st;
#pragma unroll
        for (int r = 0; r < 4; ++r) { const unsigned ub = __float_as_uint(acc2[r]); st[r] = ub == SENT ? 0x7FC00000u : ub; }
        if (fr < 8) __builtin_amdgcn_raw_buffer_store_b128(st, rin, ib_voff, (slot * INBOX_DW + (4 * w + i) * 2 * 64 * 64) * 4, AUX_SC1);
      }
    }
    STAMP(3);
    kv_store();
    STAMP(4);
#pragma unroll
    for (int i = 0; i < CH_DELAY1; ++i) __builtin_amdgcn_s_sleep(4);
    u32x4 p[2];
    {
      unsigned spins = 0;
      for (;;) {
        bool ok = true;
#pragma unroll
        for (int i = 0; i < 2; ++i) { p[i] = __builtin_amdgcn_raw_buffer_load_b128(rin, ip_voff, (slot * INBOX_DW + CT * i * 4) * 4, AUX_SC1); ok &= valid4(p[i]); }
        if (__all(ok)) break;
        if ((++spins & 63u) == 0u) {
          const unsigned t0 = __builtin_amdgcn_readfirstlane(__builtin_amdgcn_raw_buffer_load_b32(rtm, 0, 0, AUX_SC1));
          if (spins > SPIN_LIMIT || t0 == 1u) { if (lane == 0) __builtin_amdgcn_raw_buffer_store_b32(1u, rtm, 0, 0, AUX_SC1); break; }
        }
        __builtin_amdgcn_s_sleep(2);
      }
    }
    STAMP(5);
    {
      f32x4 ps;
#pragma unroll
      for (int r = 0; r < 4; ++r) ps[r] = xsum32(xsum16(__uint_as_float(p[0][r]) + __uint_as_float(p[1][r])));
      if (lane < 16) *(f32x4*)(smem + L_RS + (w * 16 + lane) * 16) = ps;
    }
    LBAR();
    if (tid < 16) {
      f32x4 t4 = *(const f32x4*)(smem + L_RS + tid * 16);
#pragma unroll
      for (int j = 1; j < 8; ++j) { const f32x4 o4 = *(const f32x4*)(smem + L_RS + (j * 16 + tid) * 16); t4 = t4 + o4; }
      u32x2 pk; pk[0] = canon(pack2(t4[0], t4[1])); pk[1] = canon(pack2(t4[2], t4[3]));
      __builtin_amdgcn_raw_buffer_store_b64(pk, rmx, (b * 32 + tid * 2) * 4, (s + 1) * MEMX_SLOT_DW * 4, AUX_SC1);
    }
    {
      const u32x4 sent = {SENT, SENT, SENT, SENT};
#pragma unroll
      for (int i = 0; i < 2; ++i) __builtin_amdgcn_raw_buffer_store_b128(sent, rin, ip_voff, (slot * INBOX_DW + CT * i * 4) * 4, AUX_SC1);
    }
    STAMP(6);
#pragma unroll
    for (int i = 0; i < CH_DELAY2; ++i) __builtin_amdgcn_s_sleep(4);
    {
      u32x4 m;
      unsigned spins = 0;
      for (;;) {
        m = __builtin_amdgcn_raw_buffer_load_b128(rmx, mp_voff, (s + 1) * MEMX_SLOT_DW * 4, AUX_SC1);
        if (__all(valid4(m))) break;
        if ((++spins & 63u) == 0u) {
          const unsigned t0 = __builtin_amdgcn_readfirstlane(__builtin_amdgcn_raw_buffer_load_b32(rtm, 0, 0, AUX_SC1));
          if (spins > SPIN_LIMIT || t0 == 1u) { if (lane == 0) __builtin_amdgcn_raw_buffer_store_b32(1u, rtm, 0, 0, AUX_SC1); break; }
        }
        __builtin_amdgcn_s_sleep(2);
      }
      *(u32x4*)(smem + L_MX + tid * 16) = m;
    }
    STAMP(7);
  }
}

__global__ __launch_bounds__(256) void attnB_kernel(const u16* __restrict__ qkvx, const u16* __restrict__ kmall, const u16* __restrict__ vmT,
                                                    const u16* __restrict__ vtx, u16* __restrict__ ox) {
  const int lane = threadIdx.x & 63, w = threadIdx.x >> 6, fr = lane & 15, g = lane >> 4;
  const int task = blockIdx.x * 4 + w, seg = task >> 5, h = task & 31;
  const u16* qrow = qkvx + (size_t)(seg * 32 + fr) * BN3 + h * 128 + 8 * g;
  bf16x8 qf[2][4];
#pragma unroll
  for (int ct = 0; ct < 2; ++ct)
#pragma unroll
    for (int ks = 0; ks < 4; ++ks) qf[ct][ks] = *(const bf16x8*)(qrow + (size_t)ct * 16 * BN3 + 32 * ks);
  f32x4 sc[2][3];
#pragma unroll
  for (int t = 0; t < 3; ++t) {
    const int key = 16 * t + fr;
    const u16* krow = key < 8 ? kmall + (size_t)(seg * 8 + key) * BHD + h * 128 + 8 * g
                              : qkvx + (size_t)(seg * 32 + (key < 40 ? key - 8 : 0)) * BN3 + BHD + h * 128 + 8 * g;
#pragma unroll
    for (int ct = 0; ct < 2; ++ct) sc[ct][t] = f32x4{0.f, 0.f, 0.f, 0.f};
#pragma unroll
    for (int ks = 0; ks < 4; ++ks) {
      const bf16x8 kf = *(const bf16x8*)(krow + 32 * ks);
#pragma unroll
      for (int ct = 0; ct < 2; ++ct) sc[ct][t] = __builtin_amdgcn_mfma_f32_16x16x32_bf16(kf, qf[ct][ks], sc[ct][t], 0, 0, 0);
    }
  }
  const float negbig = rbf(NEGBIG);
  bf16x8 pf[2][2];
#pragma unroll
  for (int ct = 0; ct < 2; ++ct) {
    const int row = BM_ + 16 * ct + fr;
    float pv[3][4]; float mx = -INFINITY;
#pragma unroll
    for (int t = 0; t < 3; ++t)
#pragma unroll
      for (int r = 0; r < 4; r += 2) {
        const unsigned c1 = cvtpk(sc[ct][t][r], sc[ct][t][r + 1]);
        const unsigned c2 = cvtpk(__uint_as_float(c1 << 16) * BSCALE, __uint_as_float(c1 & 0xffff0000u) * BSCALE);
        float v0 = __uint_as_float(c2 << 16), v1 = __uint_as_float(c2 & 0xffff0000u);
        const int key = 16 * t + 4 * g + r;
        if (key > row) v0 = negbig;
        if (key + 1 > row) v1 = negbig;
        if (t == 2 && g >= 2) { v0 = -INFINITY; v1 = -INFINITY; }
        pv[t][r] = v0; pv[t][r + 1] = v1; mx = max3f(mx, v0, v1);
      }
    mx = xmax16(mx); mx = xmax32(mx);
    float sum = 0.f;
#pragma unroll
    for (int t = 0; t < 3; ++t)
#pragma unroll
      for (int r = 0; r < 4; ++r) { pv[t][r] = __expf(pv[t][r] - mx); sum += pv[t][r]; }
    sum = xsum16(sum); sum = xsum32(sum);
    const float inv = 1.0f / sum;
    u32x4 p0, p1;
    p0[0] = cvtpk(pv[0][0] * inv, pv[0][1] * inv); p0[1] = cvtpk(pv[0][2] * inv, pv[0][3] * inv);
    p0[2] = cvtpk(pv[1][0] * inv, pv[1][1] * inv); p0[3] = cvtpk(pv[1][2] * inv, pv[1][3] * inv);
    p1[0] = cvtpk(pv[2][0] * inv, pv[2][1] * inv); p1[1] = cvtpk(pv[2][2] * inv, pv[2][3] * inv);
    p1[2] = 0u; p1[3] = 0u;
    pf[ct][0] = __builtin_bit_cast(bf16x8, p0); pf[ct][1] = __builtin_bit_cast(bf16x8, p1);
  }
  const size_t vbase = (size_t)(seg * 32 + h) * 128;
#pragma unroll
  for (int d = 0; d < 8; ++d) {
    const int dim = 16 * d + fr;
    const u16* vx = vtx + (vbase + dim) * 32;
    const u32x2 lo0 = g < 2 ? *(const u32x2*)(vmT + (vbase + dim) * 8 + 4 * g) : *(const u32x2*)(vx + 4 * (g - 2));
    const u32x2 hi0 = *(const u32x2*)(vx + 8 + 4 * g);
    const u32x2 lo1 = *(const u32x2*)(vx + 24 + 4 * (g & 1));
    const u32x4 va0 = {lo0[0], lo0[1], hi0[0], hi0[1]}, va1 = {lo1[0], lo1[1], 0u, 0u};
#pragma unroll
    for (int ct = 0; ct < 2; ++ct) {
      f32x4 oa = {0.f, 0.f, 0.f, 0.f};
      oa = __builtin_amdgcn_mfma_f32_16x16x32_bf16(__builtin_bit_cast(bf16x8, va0), pf[ct][0], oa, 0, 0, 0);
      oa = __builtin_amdgcn_mfma_f32_16x16x32_bf16(__builtin_bit_cast(bf16x8, va1), pf[ct][1], oa, 0, 0, 0);
      u32x2 pk; pk[0] = cvtpk(oa[0], oa[1]); pk[1] = cvtpk(oa[2], oa[3]);
      *(u32x2*)(ox + (size_t)(seg * 32 + 16 * ct + fr) * BHD + h * 128 + 16 * d + 4 * g) = pk;
    }
  }
}

extern "C" void kernel_launch(void* const* d_in, const int* in_sizes, int n_in,
                              void* d_out, int out_size, void* d_ws, size_t ws_size,
                              hipStream_t stream) {
  const u16* A_x = (const u16*)d_in[0];
  const u16* A_wq = (const u16*)d_in[1];
  const u16* A_wk = (const u16*)d_in[2];
  const u16* A_wv = (const u16*)d_in[3];
  const u16* A_wo = (const u16*)d_in[4];
  const u16* B_mem0 = (const u16*)d_in[6];
  const u16* B_Wq = (const u16*)d_in[7];
  const u16* B_Wk = (const u16*)d_in[8];
  const u16* B_Wv = (const u16*)d_in[9];
  const u16* B_Wo = (const u16*)d_in[10];
  u16* out = (u16*)d_out;

  char* ws = (char*)d_ws; size_t off = 0;
  auto alloc = [&](size_t bytes) { char* p = ws + off; off += (bytes + 255) & ~(size_t)255; return (u16*)p; };
  u16* xn = alloc((size_t)S_ * D_ * 2);
  u16* x1 = alloc((size_t)S_ * D_ * 2);
  u16* x2 = alloc((size_t)S_ * D_ * 2);
  u16* x3 = alloc((size_t)S_ * D_ * 2);
  u16* aqkv = alloc((size_t)S_ * AQ_LD * 2);
  u16* ao = alloc((size_t)S_ * D_ * 2);
  u16* Awqkv_t = alloc((size_t)AQ_LD * D_ * 2);
  u16* Awo_t = alloc((size_t)D_ * D_ * 2);
  u16* BWqkv_t = alloc((size_t)BN3 * D_ * 2);
  u16* BWo_t = alloc((size_t)D_ * BHD * 2);
  u16* qkvx = alloc((size_t)S_ * BN3 * 2);
  u16* ox = alloc((size_t)S_ * BHD * 2);
  u16* vtx = alloc((size_t)S_ * BHD * 2);
  u16* akvx = alloc((size_t)S_ * 32 * 2);
  u16* kmall = alloc((size_t)NSEG * 8 * BHD * 2);
  u16* vmall = alloc((size_t)NSEG * 8 * BHD * 2);
  char* exch0 = ws + off;
  unsigned* tmo = (unsigned*)alloc(256 + 64 * 8 * 8 * 8);
  unsigned* memx = (unsigned*)alloc((size_t)2 * (NSEG + 1) * MEMX_SLOT_DW * 4);
  unsigned* inbox = (unsigned*)alloc((size_t)2 * INBOX_DW * 4);
  hipMemsetAsync(exch0, 0xFF, (size_t)((ws + off) - exch0), stream);
  int blayer = 0;

  {
    TrJobs jb;
    const u16* srcs[8] = {A_wq, A_wk, A_wv, A_wo, B_Wq, B_Wk, B_Wv, B_Wo};
    u16* dsts[8] = {Awqkv_t, Awqkv_t + 512 * 512, Awqkv_t + 528 * 512, Awo_t, BWqkv_t, BWqkv_t + (size_t)4096 * 512, BWqkv_t + (size_t)8192 * 512, BWo_t};
    const int Ks[8] = {512, 512, 512, 512, 512, 512, 512, 4096}, Ns[8] = {512, 16, 16, 512, 4096, 4096, 4096, 512};
    int t0 = 0;
    for (int i = 0; i < 8; ++i) { jb.src[i] = srcs[i]; jb.dst[i] = dsts[i]; jb.K[i] = Ks[i]; jb.N[i] = Ns[i]; jb.tile0[i] = t0; t0 += (Ks[i] / 64) * ((Ns[i] + 63) / 64); }
    jb.tile0[8] = t0; jb.pad_ = 0;
    transpose_all_kernel<<<t0, 256, 0, stream>>>(jb);
  }

  auto layerA = [&](const u16* xin, u16* xout) {
    ln_kernel<<<512, 256, 0, stream>>>(xin, xn);
    gemm_nt<64, 64, 3><<<dim3(9, 32), 256, 0, stream>>>(xn, 512, Awqkv_t, 512, aqkv, AQ_LD, AQ_LD, 512, nullptr, akvx);
    attnA_kernel<<<256, 512, 0, stream>>>(aqkv, akvx, ao);
    gemm_nt<64, 64, 1><<<dim3(8, 32), 256, 0, stream>>>(ao, 512, Awo_t, 512, xout, 512, 512, 512, xin, nullptr);
  };
  auto layerB = [&](const u16* xin, u16* xout) {
    ln_kernel<<<512, 256, 0, stream>>>(xin, xn);
    gemm_nt<128, 128, 2><<<dim3(96, 16), 256, 0, stream>>>(xn, 512, BWqkv_t, 512, qkvx, BN3, BN3, 512, nullptr, vtx);
    chain_kernel<<<NCH, CT, 0, stream>>>(BWqkv_t, BWo_t, B_mem0, qkvx, vtx, kmall, vmall, memx + (size_t)blayer * (NSEG + 1) * MEMX_SLOT_DW, inbox, tmo);
    ++blayer;
    attnB_kernel<<<512, 256, 0, stream>>>(qkvx, kmall, vmall, vtx, ox);
    gemm_nt<64, 64, 1><<<dim3(8, 32), 256, 0, stream>>>(ox, BHD, BWo_t, BHD, xout, 512, 512, BHD, xin, nullptr);
  };
  layerA(A_x, x1);
  layerB(x1, x2);
  layerA(x2, x3);
  layerB(x3, out);
}
```

```cpp
#include <hip/hip_runtime.h>
#include <hip/hip_bf16.h>
#include <stdint.h>

typedef unsigned short u16;
using bf16x8 = __attribute__((ext_vector_type(8))) short;
using f32x4  = __attribute__((ext_vector_type(4))) float;
using f32x16 = __attribute__((ext_vector_type(16))) float;
using u32x4  = __attribute__((ext_vector_type(4))) unsigned;
using u32x2  = __attribute__((ext_vector_type(2))) unsigned;
#define DI __device__ __forceinline__

DI float bf2f(u16 x) { return __uint_as_float(((unsigned)x) << 16); }
DI u16 f2bf(float f) { __bf16 b = (__bf16)f; return __builtin_bit_cast(u16, b); }
DI float rbf(float f) { return bf2f(f2bf(f)); }
typedef __attribute__((ext_vector_type(2))) __bf16 bf16x2_t;
typedef __attribute__((ext_vector_type(2))) float f32x2_t;
DI unsigned cvtpk(float lo, float hi) { const f32x2_t v = {lo, hi}; return __builtin_bit_cast(unsigned, __builtin_convertvector(v, bf16x2_t)); }
DI unsigned pack2(float lo, float hi) { return cvtpk(lo, hi); }
DI float max3f(float a, float b, float c) { float r; asm("v_max3_f32 %0, %1, %2, %3" : "=v"(r) : "v"(a), "v"(b), "v"(c)); return r; }
DI float max3f_pad(float a, float b, float c) { float r; asm("v_max3_f32 %0, %1, %2, %3\n\ts_nop 1" : "=v"(r) : "v"(a), "v"(b), "v"(c)); return r; }
DI float xmax16(float x) { x = max3f_pad(x, x, x); auto r = __builtin_amdgcn_permlane16_swap(__float_as_uint(x), __float_as_uint(x), false, false); return max3f(__uint_as_float(r[0]), __uint_as_float(r[1]), __uint_as_float(r[1])); }
DI float xmax32(float x) { x = max3f_pad(x, x, x); auto r = __builtin_amdgcn_permlane32_swap(__float_as_uint(x), __float_as_uint(x), false, false); return max3f(__uint_as_float(r[0]), __uint_as_float(r[1]), __uint_as_float(r[1])); }
DI float xsum16(float x) { auto r = __builtin_amdgcn_permlane16_swap(__float_as_uint(x), __float_as_uint(x), false, false); return __uint_as_float(r[0]) + __uint_as_float(r[1]); }
DI float xsum32(float x) { auto r = __builtin_amdgcn_permlane32_swap(__float_as_uint(x), __float_as_uint(x), false, false); return __uint_as_float(r[0]) + __uint_as_float(r[1]); }

constexpr int S_ = 2048, D_ = 512;
constexpr int AH = 32, ADH = 16;
constexpr int BH = 32, BDH = 128, BM_ = 8, SEG = 32, NSEG = 64;
constexpr int BN3 = 3 * BH * BDH;
constexpr int BHD = BH * BDH;

__global__ __launch_bounds__(256) void ln_kernel(const u16* __restrict__ x, u16* __restrict__ y) {
  const int row = blockIdx.x * 4 + (threadIdx.x >> 6), lane = threadIdx.x & 63;
  const u32x4 v = *(const u32x4*)(x + (size_t)row * D_ + lane * 8);
  float f[8];
#pragma unroll
  for (int i = 0; i < 4; ++i) { f[2 * i] = __uint_as_float(v[i] << 16); f[2 * i + 1] = __uint_as_float(v[i] & 0xffff0000u); }
  float s = 0.f;
#pragma unroll
  for (int i = 0; i < 8; ++i) s += f[i];
#pragma unroll
  for (int o = 32; o >= 1; o >>= 1) s += __shfl_xor(s, o);
  const float mean = s * (1.0f / D_);
  float q = 0.f;
#pragma unroll
  for (int i = 0; i < 8; ++i) { const float d = f[i] - mean; q += d * d; }
#pragma unroll
  for (int o = 32; o >= 1; o >>= 1) q += __shfl_xor(q, o);
  const float var = q * (1.0f / D_);
  const float mb = rbf(mean), vb = rbf(var);
  const float den = rbf(sqrtf(rbf(vb + 1e-5f)));
  u32x4 o;
#pragma unroll
  for (int i = 0; i < 4; ++i) o[i] = pack2(rbf(f[2 * i] - mb) / den, rbf(f[2 * i + 1] - mb) / den);
  *(u32x4*)(y + (size_t)row * D_ + lane * 8) = o;
}

template <int NP>
__global__ __launch_bounds__(256) void finish_ln_kernel(const float* __restrict__ P, const u16* __restrict__ resid, u16* __restrict__ xo, u16* __restrict__ y) {
  const int row = blockIdx.x * 4 + (threadIdx.x >> 6), lane = threadIdx.x & 63;
  float f[8];
  {
    f32x4 a0 = *(const f32x4*)(P + (size_t)row * D_ + lane * 8), a1 = *(const f32x4*)(P + (size_t)row * D_ + lane * 8 + 4);
#pragma unroll
    for (int z = 1; z < NP; ++z) {
      const f32x4 b0 = *(const f32x4*)(P + (size_t)z * S_ * D_ + (size_t)row * D_ + lane * 8), b1 = *(const f32x4*)(P + (size_t)z * S_ * D_ + (size_t)row * D_ + lane * 8 + 4);
      a0 = a0 + b0; a1 = a1 + b1;
    }
    const u32x4 rv = *(const u32x4*)(resid + (size_t)row * D_ + lane * 8);
#pragma unroll
    for (int i = 0; i < 4; ++i) {
      const unsigned t = i < 2 ? cvtpk(a0[2 * i], a0[2 * i + 1]) : cvtpk(a1[2 * i - 4], a1[2 * i - 3]);
      const unsigned xs = cvtpk(__uint_as_float(t << 16) + __uint_as_float(rv[i] << 16), __uint_as_float(t & 0xffff0000u) + __uint_as_float(rv[i] & 0xffff0000u));
      f[2 * i] = __uint_as_float(xs << 16); f[2 * i + 1] = __uint_as_float(xs & 0xffff0000u);
    }
    u32x4 o;
#pragma unroll
    for (int i = 0; i < 4; ++i) o[i] = cvtpk(f[2 * i], f[2 * i + 1]);
    *(u32x4*)(xo + (size_t)row * D_ + lane * 8) = o;
  }
  if (y == nullptr) return;
  float s = 0.f;
#pragma unroll
  for (int i = 0; i < 8; ++i) s += f[i];
#pragma unroll
  for (int o = 32; o >= 1; o >>= 1) s += __shfl_xor(s, o);
  const float mean = s * (1.0f / D_);
  float q = 0.f;
#pragma unroll
  for (int i = 0; i < 8; ++i) { const float d = f[i] - mean; q += d * d; }
#pragma unroll
  for (int o = 32; o >= 1; o >>= 1) q += __shfl_xor(q, o);
  const float var = q * (1.0f / D_);
  const float mb = rbf(mean), vb = rbf(var);
  const float den = rbf(sqrtf(rbf(vb + 1e-5f)));
  u32x4 o;
#pragma unroll
  for (int i = 0; i < 4; ++i) o[i] = pack2(rbf(f[2 * i] - mb) / den, rbf(f[2 * i + 1] - mb) / den);
  *(u32x4*)(y + (size_t)row * D_ + lane * 8) = o;
}

struct TrJobs { const u16* src[8]; u16* dst[8]; int K[8]; int N[8]; int tile0[9]; int pad_; };
__global__ __launch_bounds__(256) void transpose_all_kernel(TrJobs jobs) {
  __shared__ __attribute__((aligned(16))) u16 t[64][68];
  int j = 0;
#pragma unroll
  for (int i = 1; i < 8; ++i) j += (int)blockIdx.x >= jobs.tile0[i];
  const int K = jobs.K[j], N = jobs.N[j], tl = blockIdx.x - jobs.tile0[j];
  const int ntn = (N + 63) >> 6, n0 = (tl % ntn) * 64, k0 = (tl / ntn) * 64;
  const u16* __restrict__ in = jobs.src[j]; u16* __restrict__ out = jobs.dst[j];
  const int tx = threadIdx.x & 15, ty = threadIdx.x >> 4;
#pragma unroll
  for (int i = 0; i < 4; ++i) {
    const int r = ty + 16 * i, c = tx * 4;
    if (n0 + c < N) {
      const u32x2 v = *(const u32x2*)(in + (size_t)(k0 + r) * N + n0 + c);
      t[c][r] = (u16)(v[0] & 0xffff); t[c + 1][r] = (u16)(v[0] >> 16); t[c + 2][r] = (u16)(v[1] & 0xffff); t[c + 3][r] = (u16)(v[1] >> 16);
    }
  }
  __syncthreads();
#pragma unroll
  for (int i = 0; i < 4; ++i) {
    const int n = ty + 16 * i;
    if (n0 + n < N) *(u32x2*)(out + (size_t)(n0 + n) * K + k0 + tx * 4) = *(const u32x2*)(&t[n][tx * 4]);
  }
}

template <int BM, int BN, int EPI>
__global__ __launch_bounds__(256) void gemm_nt(const u16* __restrict__ A, int lda, const u16* __restrict__ Bt, int ldb,
                                               u16* __restrict__ C, int ldc, int N, int K, const u16* __restrict__ R, u16* __restrict__ X) {
  constexpr int BK = 64, LS = 72;
  constexpr int MT = BM / 32, NT = BN / 32;
  constexpr int CA = BM * 8 / 256, CB = BN * 8 / 256;
  constexpr int LDS_AB = (BM + BN) * LS * 2, LDS_C = EPI == 4 ? BM * (BN + 4) * 4 : BM * (BN + 8) * 2;
  __shared__ __attribute__((aligned(16))) char smem[LDS_AB > LDS_C ? LDS_AB : LDS_C];
  u16* As = (u16*)smem; u16* Bs = As + BM * LS;
  const int tid = threadIdx.x, lane = tid & 63, w = tid >> 6, wm = w >> 1, wn = w & 1;
  const int m0 = blockIdx.y * BM, n0 = blockIdx.x * BN;
  const int fr = lane & 15, fq = lane >> 4;
  if (EPI == 4) { A += (size_t)blockIdx.z * K; Bt += (size_t)blockIdx.z * K; }
  f32x4 acc[MT][NT];
#pragma unroll
  for (int i = 0; i < MT; ++i)
#pragma unroll
    for (int j = 0; j < NT; ++j) acc[i][j] = f32x4{0.f, 0.f, 0.f, 0.f};
  u32x4 ra[CA], rb[CB];
  auto gload = [&](int k0) {
#pragma unroll
    for (int i = 0; i < CA; ++i) { const int c = tid + i * 256, r = c >> 3, c8 = c & 7; ra[i] = *(const u32x4*)(A + (size_t)(m0 + r) * lda + k0 + c8 * 8); }
#pragma unroll
    for (int i = 0; i < CB; ++i) { const int c = tid + i * 256, r = c >> 3, c8 = c & 7; int n = n0 + r; n = n < N ? n : N - 1;
      rb[i] = *(const u32x4*)(Bt + (size_t)n * ldb + k0 + c8 * 8); }
  };
  gload(0);
  for (int k0 = 0; k0 < K; k0 += BK) {
    __syncthreads();
#pragma unroll
    for (int i = 0; i < CA; ++i) { const int c = tid + i * 256, r = c >> 3, c8 = c & 7; *(u32x4*)(As + r * LS + c8 * 8) = ra[i]; }
#pragma unroll
    for (int i = 0; i < CB; ++i) { const int c = tid + i * 256, r = c >> 3, c8 = c & 7; *(u32x4*)(Bs + r * LS + c8 * 8) = rb[i]; }
    __syncthreads();
    if (k0 + BK < K) gload(k0 + BK);
#pragma unroll
    for (int kk = 0; kk < 2; ++kk) {
      bf16x8 af[MT], bfr[NT];
#pragma unroll
      for (int i = 0; i < MT; ++i) af[i] = *(const bf16x8*)(As + (wm * (BM / 2) + i * 16 + fr) * LS + kk * 32 + fq * 8);
#pragma unroll
      for (int j = 0; j < NT; ++j) bfr[j] = *(const bf16x8*)(Bs + (wn * (BN / 2) + j * 16 + fr) * LS + kk * 32 + fq * 8);
#pragma unroll
      for (int i = 0; i < MT; ++i)
#pragma unroll
        for (int j = 0; j < NT; ++j) acc[i][j] = __builtin_amdgcn_mfma_f32_16x16x32_bf16(af[i], bfr[j], acc[i][j], 0, 0, 0);
    }
  }
  if ((EPI == 2 && n0 >= 2 * BHD) || (EPI == 3 && n0 + BN > 512)) {
#pragma unroll
    for (int i = 0; i < MT; ++i)
#pragma unroll
      for (int j = 0; j < NT; ++j) {
        const int n = n0 + wn * (BN / 2) + j * 16 + fr, m = m0 + wm * (BM / 2) + i * 16 + fq * 4;
        u32x2 pk; pk[0] = pack2(acc[i][j][0], acc[i][j][1]); pk[1] = pack2(acc[i][j][2], acc[i][j][3]);
        if (EPI == 2) {
          const int hh = (n - 2 * BHD) >> 7, dim = n & 127;
          *(u32x2*)(X + ((size_t)((m >> 5) * 32 + hh) * 128 + dim) * 32 + (m & 31)) = pk;
        } else if (n >= 512 && n < 528) {
#pragma unroll
          for (int r = 0; r < 4; ++r) X[(size_t)(m + r) * 16 + (n - 512)] = (u16)(r & 1 ? pk[r >> 1] >> 16 : pk[r >> 1] & 0xffff);
        } else if (n >= 528 && n < 544) {
          const int pos = (m & ~15) | ((fq & 1) * 8 + (fq >> 1) * 4);
          *(u32x2*)(X + 2048 * 16 + (size_t)(n - 528) * 2048 + pos) = pk;
        } else if (n < 512) {
#pragma unroll
          for (int r = 0; r < 4; ++r) C[(size_t)(m + r) * ldc + n] = (u16)(r & 1 ? pk[r >> 1] >> 16 : pk[r >> 1] & 0xffff);
        }
      }
    return;
  }
  if (EPI == 4) {
    __syncthreads();
    constexpr int FS = BN + 4;
    float* Fs = (float*)smem;
#pragma unroll
    for (int i = 0; i < MT; ++i)
#pragma unroll
      for (int j = 0; j < NT; ++j)
#pragma unroll
        for (int r = 0; r < 4; ++r) Fs[(wm * (BM / 2) + i * 16 + fq * 4 + r) * FS + wn * (BN / 2) + j * 16 + fr] = acc[i][j][r];
    __syncthreads();
    float* Pz = (float*)X + (size_t)blockIdx.z * S_ * ldc;
    for (int c = tid; c < BM * BN / 4; c += 256) { const int r = c / (BN / 4), c4 = c % (BN / 4);
      *(f32x4*)(Pz + (size_t)(m0 + r) * ldc + n0 + c4 * 4) = *(const f32x4*)(Fs + r * FS + c4 * 4); }
    return;
  }
  __syncthreads();
  constexpr int CS = BN + 8;
  u16* Cs = (u16*)smem;
#pragma unroll
  for (int i = 0; i < MT; ++i)
#pragma unroll
    for (int j = 0; j < NT; ++j)
#pragma unroll
      for (int r = 0; r < 4; ++r) Cs[(wm * (BM / 2) + i * 16 + fq * 4 + r) * CS + wn * (BN / 2) + j * 16 + fr] = f2bf(acc[i][j][r]);
  __syncthreads();
  for (int c = tid; c < BM * BN / 8; c += 256) {
    const int r = c / (BN / 8), c8 = c % (BN / 8);
    const int n = n0 + c8 * 8;
    if (n >= N) continue;
    u32x4 v = *(const u32x4*)(Cs + r * CS + c8 * 8);
    if (EPI == 1) {
      const u32x4 rr = *(const u32x4*)(R + (size_t)(m0 + r) * ldc + n);
#pragma unroll
      for (int i = 0; i < 4; ++i) {
        const float lo = bf2f((u16)(v[i] & 0xffff)) + bf2f((u16)(rr[i] & 0xffff));
        const float hi = bf2f((u16)(v[i] >> 16)) + bf2f((u16)(rr[i] >> 16));
        v[i] = pack2(lo, hi);
      }
    }
    *(u32x4*)(C + (size_t)(m0 + r) * ldc + n) = v;
  }
}

constexpr int AQ_LD = 544;
constexpr int VT_LD = 2048 + 8;
__global__ __launch_bounds__(512) void attnA_kernel(const u16* __restrict__ qkv, const u16* __restrict__ kvx, u16* __restrict__ o) {
  __shared__ __attribute__((aligned(16))) u16 Ks[2048 * 16];
  __shared__ __attribute__((aligned(16))) u16 Vt[16 * VT_LD];
  const int tid = threadIdx.x, lane = tid & 63, w = tid >> 6;
  for (int c = tid; c < 2048 * 2; c += 512) *(u32x4*)(Ks + c * 8) = *(const u32x4*)(kvx + c * 8);
  for (int c = tid; c < 16 * 256; c += 512) { const int d = c >> 8, k8 = c & 255; *(u32x4*)(Vt + d * VT_LD + k8 * 8) = *(const u32x4*)(kvx + 2048 * 16 + d * 2048 + k8 * 8); }
  __syncthreads();
  const int tok = blockIdx.x * 8 + w;
  const int c32 = lane & 31, hh = lane >> 5;
  const bf16x8 qb = *(const bf16x8*)(qkv + (size_t)tok * AQ_LD + c32 * 16 + hh * 8);
  const float cs = 0.25f * 1.4426950408889634f;
  float m_run = -INFINITY, l_run = 0.f;
  f32x16 oacc;
#pragma unroll
  for (int i = 0; i < 16; ++i) oacc[i] = 0.f;
  const f32x16 zero16 = oacc;
  const u16* kp = Ks + c32 * 16 + hh * 8;
  const u16* vp = Vt + (lane & 15) * VT_LD + hh * 8;
  auto tile = [&](f32x16& x, int t) {
#pragma unroll
    for (int i = 0; i < 16; i += 2) { const unsigned c = cvtpk(x[i], x[i + 1]); x[i] = __uint_as_float(c << 16); x[i + 1] = __uint_as_float(c & 0xffff0000u); }
    float mt = max3f(x[0], x[1], x[2]);
#pragma unroll
    for (int i = 3; i < 15; i += 2) mt = max3f(mt, x[i], x[i + 1]);
    mt = max3f(mt, x[15], x[15]);
    mt = xmax32(mt);
    const float m_new = max3f(m_run, mt, mt);
    const float alpha = __builtin_amdgcn_exp2f((m_run - m_new) * cs);
    const float mc = m_new * cs;
    float ls = 0.f;
    u32x4 pk0, pk1;
#pragma unroll
    for (int i = 0; i < 16; i += 2) {
      const float e0 = __builtin_amdgcn_exp2f(__builtin_fmaf(x[i], cs, -mc)), e1 = __builtin_amdgcn_exp2f(__builtin_fmaf(x[i + 1], cs, -mc));
      ls += e0 + e1;
      const unsigned pk = cvtpk(e0, e1);
      if (i < 8) pk0[i >> 1] = pk; else pk1[(i - 8) >> 1] = pk;
    }
    l_run = __builtin_fmaf(l_run, alpha, ls);
    m_run = m_new;
#pragma unroll
    for (int i = 0; i < 8; ++i) oacc[i] *= alpha;
    const bf16x8 va0 = *(const bf16x8*)(vp + t * 32), va1 = *(const bf16x8*)(vp + t * 32 + 16);
    oacc = __builtin_amdgcn_mfma_f32_32x32x16_bf16(va0, __builtin_bit_cast(bf16x8, pk0), oacc, 0, 0, 0);
    oacc = __builtin_amdgcn_mfma_f32_32x32x16_bf16(va1, __builtin_bit_cast(bf16x8, pk1), oacc, 0, 0, 0);
  };
  f32x16 xa = __builtin_amdgcn_mfma_f32_32x32x16_bf16(*(const bf16x8*)(kp), qb, zero16, 0, 0, 0), xb;
  for (int t = 0; t < 64; t += 2) {
    xb = __builtin_amdgcn_mfma_f32_32x32x16_bf16(*(const bf16x8*)(kp + (t + 1) * 32 * 16), qb, zero16, 0, 0, 0);
    tile(xa, t);
    if (t + 2 < 64) xa = __builtin_amdgcn_mfma_f32_32x32x16_bf16(*(const bf16x8*)(kp + (t + 2) * 32 * 16), qb, zero16, 0, 0, 0);
    tile(xb, t + 1);
  }
  l_run += __shfl_xor(l_run, 32);
  const float inv = 1.0f / l_run;
  u32x2 o0, o1;
  o0[0] = pack2(oacc[0] * inv, oacc[1] * inv); o0[1] = pack2(oacc[2] * inv, oacc[3] * inv);
  o1[0] = pack2(oacc[4] * inv, oacc[5] * inv); o1[1] = pack2(oacc[6] * inv, oacc[7] * inv);
  u16* op = o + (size_t)tok * 512 + c32 * 16;
  *(u32x2*)(op + 4 * hh) = o0;
  *(u32x2*)(op + 8 + 4 * hh) = o1;
}

DI float red16_max(float v) { v = fmaxf(v, __shfl_xor(v, 1)); v = fmaxf(v, __shfl_xor(v, 2)); v = fmaxf(v, __shfl_xor(v, 4)); v = fmaxf(v, __shfl_xor(v, 8)); return v; }
DI float red16_sum(float v) { v += __shfl_xor(v, 1); v += __shfl_xor(v, 2); v += __shfl_xor(v, 4); v += __shfl_xor(v, 8); return v; }

constexpr float BSCALE = 0.08838834764831845f;
constexpr float NEGBIG = -1000000000.0f;

constexpr unsigned SENT = 0xFFFFFFFFu;
#define AUX_SC1 16
constexpr int KS_LD = 136, VT_LDB = 40, OS_LD = 72;
constexpr int NCH = 64;
constexpr int L_WV = 0, L_WV_BYTES = 4 * 16 * 1024;
constexpr int L_WO = L_WV + L_WV_BYTES, L_WO_BYTES = 32 * 2 * 1024;
constexpr int L_VT = L_WO + L_WO_BYTES, L_VT_BYTES = 64 * VT_LDB * 2;
constexpr int L_K = L_VT + L_VT_BYTES, L_K_BYTES = 40 * KS_LD * 2;
constexpr int L_Q = L_K + L_K_BYTES, L_Q_BYTES = 8 * KS_LD * 2;
constexpr int L_MX = L_Q + L_Q_BYTES, L_MX_BYTES = 8192;
constexpr int L_O = L_MX;
constexpr int L_RS = L_MX + L_MX_BYTES;
constexpr int L_TOTAL = L_RS + 2048;
constexpr int MEMX_SLOT_DW = 64 * 32;
constexpr int INBOX_DW = 64 * 64 * 64;
constexpr unsigned SPIN_LIMIT = 1u << 15;
#define CH_DELAY1 0
#define CH_DELAY2 4

#define STAMP(i)
DI bool valid4(u32x4 v) { return v[0] != SENT && v[1] != SENT && v[2] != SENT && v[3] != SENT; }
DI unsigned canon(unsigned x) { return x == SENT ? 0x7FC07FC0u : x; }

#define LBAR() asm volatile("s_waitcnt lgkmcnt(0)\n\ts_barrier" ::: "memory")
constexpr int CT = 512;

constexpr int CNT_KV = 0, CNT_Q = 16, CNT_SEG = 32, CNT_O = 96, CNT_LINES = 112;
struct BArgs {
  const u16* xn; const u16* Wqkv_t; const u16* Wo_t; const u16* mem0;
  u16* qkvx; u16* vtx; u16* kmall; u16* vmT; u16* ox; float* part;
  unsigned* memx; unsigned* inbox; unsigned* tmo; unsigned* cnt;
};
DI void cnt_add(unsigned* c) { __hip_atomic_fetch_add(c, 1u, __ATOMIC_RELAXED, __HIP_MEMORY_SCOPE_AGENT); }
DI void cnt_wait(__amdgpu_buffer_rsrc_t rc, int line, unsigned n, __amdgpu_buffer_rsrc_t rtm) {
  for (unsigned spins = 0;;) {
    const unsigned v = __builtin_amdgcn_readfirstlane(__builtin_amdgcn_raw_buffer_load_b32(rc, line * 128, 0, AUX_SC1));
    if (v + 1u >= n) return;
    if ((++spins & 15u) == 0u) {
      const unsigned t0 = __builtin_amdgcn_readfirstlane(__builtin_amdgcn_raw_buffer_load_b32(rtm, 0, 0, AUX_SC1));
      if (spins > (SPIN_LIMIT << 2) || t0 == 1u) { __builtin_amdgcn_raw_buffer_store_b32(1u, rtm, 0, 0, AUX_SC1); return; }
    }
    if (spins < 4) __builtin_amdgcn_s_sleep(8); else __builtin_amdgcn_s_sleep(48);
  }
}
#define VMDRAIN() asm volatile("s_waitcnt vmcnt(0)" ::: "memory")

template <int BN, int MODE>
DI void helper_gemm_tile(const BArgs& p, char* smem, int m0, int n0, int kz, int tid) {
  constexpr int BM = 128, LS = 72;
  constexpr int WGN = BN / 32;
  constexpr int WGM = 8 / WGN, WM = BM / WGM;
  constexpr int MT = WM / 16, NT = 2;
  constexpr int K = MODE == 0 ? 512 : 1024;
  constexpr int CA = BM * 8 / CT, CB = (BN * 8 + CT - 1) / CT;
  u16* As = (u16*)smem; u16* Bs = As + BM * LS;
  const int lane = tid & 63, w = tid >> 6, wm = w / WGN, wn = w % WGN, fr = lane & 15, fq = lane >> 4;
  const u16* __restrict__ Bt = (MODE == 0 ? p.Wqkv_t : p.Wo_t) + (MODE == 1 ? kz * K : 0);
  constexpr int ldb = MODE == 0 ? 512 : BHD;
  const __amdgpu_buffer_rsrc_t rox = __builtin_amdgcn_make_buffer_rsrc((void*)p.ox, 0, S_ * BHD * 2, 0x00020000);
  f32x4 acc[MT][NT];
#pragma unroll
  for (int i = 0; i < MT; ++i)
#pragma unroll
    for (int j = 0; j < NT; ++j) acc[i][j] = f32x4{0.f, 0.f, 0.f, 0.f};
  u32x4 ra[CA], rb[CB];
  auto gload = [&](int k0) {
#pragma unroll
    for (int i = 0; i < CA; ++i) { const int c = tid + i * CT, r = c >> 3, c8 = c & 7;
      if (MODE == 0) ra[i] = *(const u32x4*)(p.xn + (size_t)(m0 + r) * 512 + k0 + c8 * 8);
      else ra[i] = __builtin_amdgcn_raw_buffer_load_b128(rox, ((m0 + r) * BHD + kz * K + k0 + c8 * 8) * 2, 0, AUX_SC1); }
#pragma unroll
    for (int i = 0; i < CB; ++i) { const int c = tid + i * CT, r = c >> 3, c8 = c & 7;
      if (c < BN * 8) rb[i] = *(const u32x4*)(Bt + (size_t)(n0 + r) * ldb + k0 + c8 * 8); }
  };
  gload(0);
  for (int k0 = 0; k0 < K; k0 += 64) {
    __syncthreads();
#pragma unroll
    for (int i = 0; i < CA; ++i) { const int c = tid + i * CT, r = c >> 3, c8 = c & 7; *(u32x4*)(As + r * LS + c8 * 8) = ra[i]; }
#pragma unroll
    for (int i = 0; i < CB; ++i) { const int c = tid + i * CT, r = c >> 3, c8 = c & 7; if (c < BN * 8) *(u32x4*)(Bs + r * LS + c8 * 8) = rb[i]; }
    __syncthreads();
    if (k0 + 64 < K) gload(k0 + 64);
#pragma unroll
    for (int kk = 0; kk < 2; ++kk) {
      bf16x8 af[MT], bfr[NT];
#pragma unroll
      for (int i = 0; i < MT; ++i) af[i] = *(const bf16x8*)(As + (wm * WM + i * 16 + fr) * LS + kk * 32 + fq * 8);
#pragma unroll
      for (int j = 0; j < NT; ++j) bfr[j] = *(const bf16x8*)(Bs + (wn * 32 + j * 16 + fr) * LS + kk * 32 + fq * 8);
#pragma unroll
      for (int i = 0; i < MT; ++i)
#pragma unroll
        for (int j = 0; j < NT; ++j) acc[i][j] = __builtin_amdgcn_mfma_f32_16x16x32_bf16(af[i], bfr[j], acc[i][j], 0, 0, 0);
    }
  }
  __syncthreads();
  if (MODE == 0) {
    if (n0 >= 2 * BHD) {
      const __amdgpu_buffer_rsrc_t rv = __builtin_amdgcn_make_buffer_rsrc((void*)p.vtx, 0, S_ * BHD * 2, 0x00020000);
#pragma unroll
      for (int i = 0; i < MT; ++i)
#pragma unroll
        for (int j = 0; j < NT; ++j) {
          const int n = n0 + wn * 32 + j * 16 + fr, m = m0 + wm * WM + i * 16 + fq * 4;
          u32x2 pk; pk[0] = pack2(acc[i][j][0], acc[i][j][1]); pk[1] = pack2(acc[i][j][2], acc[i][j][3]);
          __builtin_amdgcn_raw_buffer_store_b64(pk, rv, ((((m >> 5) * 32 + ((n - 2 * BHD) >> 7)) * 128 + (n & 127)) * 32 + (m & 31)) * 2, 0, AUX_SC1);
        }
    } else {
      constexpr int CS = BN + 8;
      u16* Cs = (u16*)smem;
#pragma unroll
      for (int i = 0; i < MT; ++i)
#pragma unroll
        for (int j = 0; j < NT; ++j)
#pragma unroll
          for (int r = 0; r < 4; ++r) Cs[(wm * WM + i * 16 + fq * 4 + r) * CS + wn * 32 + j * 16 + fr] = f2bf(acc[i][j][r]);
      __syncthreads();
      const __amdgpu_buffer_rsrc_t rq = __builtin_amdgcn_make_buffer_rsrc((void*)p.qkvx, 0, S_ * BN3 * 2, 0x00020000);
      for (int c = tid; c < BM * BN / 8; c += CT) { const int r = c / (BN / 8), c8 = c % (BN / 8);
        __builtin_amdgcn_raw_buffer_store_b128(*(const u32x4*)(Cs + r * CS + c8 * 8), rq, ((m0 + r) * BN3 + n0 + c8 * 8) * 2, 0, AUX_SC1); }
    }
  } else {
    constexpr int FS = BN + 4;
    float* Fs = (float*)smem;
#pragma unroll
    for (int i = 0; i < MT; ++i)
#pragma unroll
      for (int j = 0; j < NT; ++j)
#pragma unroll
        for (int r = 0; r < 4; ++r) Fs[(wm * WM + i * 16 + fq * 4 + r) * FS + wn * 32 + j * 16 + fr] = acc[i][j][r];
    __syncthreads();
    float* Pz = p.part + (size_t)kz * S_ * D_;
    for (int c = tid; c < BM * BN / 4; c += CT) { const int r = c / (BN / 4), c4 = c % (BN / 4);
      *(f32x4*)(Pz + (size_t)(m0 + r) * D_ + n0 + c4 * 4) = *(const f32x4*)(Fs + r * FS + c4 * 4); }
  }
}

DI void attn_task(const BArgs& p, int seg, int h, int lane) {
  const int fr = lane & 15, g = lane >> 4;
  const __amdgpu_buffer_rsrc_t rq = __builtin_amdgcn_make_buffer_rsrc((void*)p.qkvx, 0, S_ * BN3 * 2, 0x00020000);
  const __amdgpu_buffer_rsrc_t rk = __builtin_amdgcn_make_buffer_rsrc((void*)p.kmall, 0, NSEG * 8 * BHD * 2, 0x00020000);
  const __amdgpu_buffer_rsrc_t rvx = __builtin_amdgcn_make_buffer_rsrc((void*)p.vtx, 0, S_ * BHD * 2, 0x00020000);
  const __amdgpu_buffer_rsrc_t rvm = __builtin_amdgcn_make_buffer_rsrc((void*)p.vmT, 0, NSEG * 8 * BHD * 2, 0x00020000);
  const __amdgpu_buffer_rsrc_t rox = __builtin_amdgcn_make_buffer_rsrc((void*)p.ox, 0, S_ * BHD * 2, 0x00020000);
  bf16x8 qf[2][4];
#pragma unroll
  for (int ct = 0; ct < 2; ++ct)
#pragma unroll
    for (int ks = 0; ks < 4; ++ks)
      qf[ct][ks] = __builtin_bit_cast(bf16x8, __builtin_amdgcn_raw_buffer_load_b128(rq, ((seg * 32 + 16 * ct + fr) * BN3 + h * 128 + 8 * g + 32 * ks) * 2, 0, AUX_SC1));
  f32x4 sc[2][3];
#pragma unroll
  for (int t = 0; t < 3; ++t) {
    const int key = 16 * t + fr;
#pragma unroll
    for (int ct = 0; ct < 2; ++ct) sc[ct][t] = f32x4{0.f, 0.f, 0.f, 0.f};
#pragma unroll
    for (int ks = 0; ks < 4; ++ks) {
      u32x4 kv;
      if (key < 8) kv = __builtin_amdgcn_raw_buffer_load_b128(rk, ((seg * 8 + key) * BHD + h * 128 + 8 * g + 32 * ks) * 2, 0, AUX_SC1);
      else kv = __builtin_amdgcn_raw_buffer_load_b128(rq, ((seg * 32 + (key < 40 ? key - 8 : 0)) * BN3 + BHD + h * 128 + 8 * g + 32 * ks) * 2, 0, AUX_SC1);
      const bf16x8 kf = __builtin_bit_cast(bf16x8, kv);
#pragma unroll
      for (int ct = 0; ct < 2; ++ct) sc[ct][t] = __builtin_amdgcn_mfma_f32_16x16x32_bf16(kf, qf[ct][ks], sc[ct][t], 0, 0, 0);
    }
  }
  const float negbig = rbf(NEGBIG);
  bf16x8 pf[2][2];
#pragma unroll
  for (int ct = 0; ct < 2; ++ct) {
    const int row = BM_ + 16 * ct + fr;
    float pv[3][4]; float mx = -INFINITY;
#pragma unroll
    for (int t = 0; t < 3; ++t)
#pragma unroll
      for (int r = 0; r < 4; r += 2) {
        const unsigned c1 = cvtpk(sc[ct][t][r], sc[ct][t][r + 1]);
        const unsigned c2 = cvtpk(__uint_as_float(c1 << 16) * BSCALE, __uint_as_float(c1 & 0xffff0000u) * BSCALE);
        float v0 = __uint_as_float(c2 << 16), v1 = __uint_as_float(c2 & 0xffff0000u);
        const int key = 16 * t + 4 * g + r;
        if (key > row) v0 = negbig;
        if (key + 1 > row) v1 = negbig;
        if (t == 2 && g >= 2) { v0 = -INFINITY; v1 = -INFINITY; }
        pv[t][r] = v0; pv[t][r + 1] = v1; mx = max3f(mx, v0, v1);
      }
    mx = xmax16(mx); mx = xmax32(mx);
    float sum = 0.f;
#pragma unroll
    for (int t = 0; t < 3; ++t)
#pragma unroll
      for (int r = 0; r < 4; ++r) { pv[t][r] = __expf(pv[t][r] - mx); sum += pv[t][r]; }
    sum = xsum16(sum); sum = xsum32(sum);
    const float inv = 1.0f / sum;
    u32x4 p0, p1;
    p0[0] = cvtpk(pv[0][0] * inv, pv[0][1] * inv); p0[1] = cvtpk(pv[0][2] * inv, pv[0][3] * inv);
    p0[2] = cvtpk(pv[1][0] * inv, pv[1][1] * inv); p0[3] = cvtpk(pv[1][2] * inv, pv[1][3] * inv);
    p1[0] = cvtpk(pv[2][0] * inv, pv[2][1] * inv); p1[1] = cvtpk(pv[2][2] * inv, pv[2][3] * inv);
    p1[2] = 0u; p1[3] = 0u;
    pf[ct][0] = __builtin_bit_cast(bf16x8, p0); pf[ct][1] = __builtin_bit_cast(bf16x8, p1);
  }
  const int vb = (seg * 32 + h) * 128;
#pragma unroll
  for (int d = 0; d < 8; ++d) {
    const int dim = 16 * d + fr;
    const int vxo = (vb + dim) * 32 * 2;
    const u32x2 lo0 = g < 2 ? __builtin_amdgcn_raw_buffer_load_b64(rvm, ((vb + dim) * 8 + 4 * g) * 2, 0, AUX_SC1)
                            : __builtin_amdgcn_raw_buffer_load_b64(rvx, vxo + 8 * (g - 2), 0, AUX_SC1);
    const u32x2 hi0 = __builtin_amdgcn_raw_buffer_load_b64(rvx, vxo + (8 + 4 * g) * 2, 0, AUX_SC1);
    const u32x2 lo1 = __builtin_amdgcn_raw_buffer_load_b64(rvx, vxo + (24 + 4 * (g & 1)) * 2, 0, AUX_SC1);
    const u32x4 va0 = {lo0[0], lo0[1], hi0[0], hi0[1]}, va1 = {lo1[0], lo1[1], 0u, 0u};
#pragma unroll
    for (int ct = 0; ct < 2; ++ct) {
      f32x4 oa = {0.f, 0.f, 0.f, 0.f};
      oa = __builtin_amdgcn_mfma_f32_16x16x32_bf16(__builtin_bit_cast(bf16x8, va0), pf[ct][0], oa, 0, 0, 0);
      oa = __builtin_amdgcn_mfma_f32_16x16x32_bf16(__builtin_bit_cast(bf16x8, va1), pf[ct][1], oa, 0, 0, 0);
      u32x2 pk; pk[0] = cvtpk(oa[0], oa[1]); pk[1] = cvtpk(oa[2], oa[3]);
      __builtin_amdgcn_raw_buffer_store_b64(pk, rox, ((seg * 32 + 16 * ct + fr) * BHD + h * 128 + 16 * d + 4 * g) * 2, 0, AUX_SC1);
    }
  }
}

constexpr int NHELP = 192;
DI void helper_main(const BArgs& p, char* smem, int hid, int tid) {
  const int lane = tid & 63, w = __builtin_amdgcn_readfirstlane(tid >> 6);
  const __amdgpu_buffer_rsrc_t rc = __builtin_amdgcn_make_buffer_rsrc((void*)p.cnt, 0, CNT_LINES * 128, 0x00020000);
  const __amdgpu_buffer_rsrc_t rtm = __builtin_amdgcn_make_buffer_rsrc((void*)p.tmo, 0, 64, 0x00020000);
  for (int t = hid; t < 1536; t += NHELP) {
    int rb, cb;
    if (t < 1024) { rb = t >> 6; cb = 32 + (t & 63); } else { rb = (t - 1024) >> 5; cb = (t - 1024) & 31; }
    helper_gemm_tile<128, 0>(p, smem, rb * 128, cb * 128, 0, tid);
    VMDRAIN();
    __syncthreads();
    if (tid == 0) cnt_add(p.cnt + (t < 1024 ? CNT_KV + rb : CNT_Q + rb) * 32);
  }
  for (int t8 = hid; t8 < 256; t8 += NHELP) {
    const int seg = t8 >> 2, h = (t8 & 3) * 8 + w, rb = seg >> 2;
    if (w == 0) { cnt_wait(rc, CNT_KV + rb, 64, rtm); cnt_wait(rc, CNT_Q + rb, 32, rtm); cnt_wait(rc, CNT_SEG + seg, NCH, rtm); }
    __syncthreads();
    attn_task(p, seg, h, lane);
    VMDRAIN();
    if (lane == 0) cnt_add(p.cnt + (CNT_O + rb) * 32);
  }
  for (int it = hid; it < 512; it += NHELP) {
    const int rb = it >> 5, nb = (it >> 2) & 7, kz = it & 3;
    if (w == 0) cnt_wait(rc, CNT_O + rb, 128, rtm);
    __syncthreads();
    helper_gemm_tile<64, 1>(p, smem, rb * 128, nb * 64, kz, tid);
  }
}

__global__ __launch_bounds__(CT, 2) void blayer_kernel(BArgs p) {
  __shared__ __attribute__((aligned(16))) char smem[L_TOTAL];
  if (blockIdx.x >= NCH) { helper_main(p, smem, blockIdx.x - NCH, threadIdx.x); return; }
  const u16* __restrict__ Wqkv_t = p.Wqkv_t; const u16* __restrict__ Wo_t = p.Wo_t; const u16* __restrict__ mem0 = p.mem0;
  const u16* qkvx = p.qkvx; const u16* vtx = p.vtx; u16* kmall = p.kmall; u16* vmTall = p.vmT;
  unsigned* memx = p.memx; unsigned* inbox = p.inbox; unsigned* tmo = p.tmo;
  const int b = blockIdx.x, h = b >> 1, a = b & 1;
  const int tid = threadIdx.x, lane = tid & 63, w = __builtin_amdgcn_readfirstlane(tid >> 6), fr = lane & 15, g = lane >> 4;
  u16* VT = (u16*)(smem + L_VT); u16* KALL = (u16*)(smem + L_K); u16* QS = (u16*)(smem + L_Q); u16* OS = (u16*)(smem + L_O);
  const __amdgpu_buffer_rsrc_t rmx = __builtin_amdgcn_make_buffer_rsrc((void*)memx, 0, (NSEG + 1) * MEMX_SLOT_DW * 4, 0x00020000);
  const __amdgpu_buffer_rsrc_t rin = __builtin_amdgcn_make_buffer_rsrc((void*)inbox, 0, 2 * INBOX_DW * 4, 0x00020000);
  const __amdgpu_buffer_rsrc_t rtm = __builtin_amdgcn_make_buffer_rsrc((void*)tmo, 0, 64, 0x00020000);
  const __amdgpu_buffer_rsrc_t rqk = __builtin_amdgcn_make_buffer_rsrc((void*)qkvx, 0, S_ * BN3 * 2, 0x00020000);
  const __amdgpu_buffer_rsrc_t rvx = __builtin_amdgcn_make_buffer_rsrc((void*)vtx, 0, S_ * BHD * 2, 0x00020000);
  const __amdgpu_buffer_rsrc_t rkm = __builtin_amdgcn_make_buffer_rsrc((void*)kmall, 0, NSEG * 8 * BHD * 2, 0x00020000);
  const __amdgpu_buffer_rsrc_t rvm = __builtin_amdgcn_make_buffer_rsrc((void*)vmTall, 0, NSEG * 8 * BHD * 2, 0x00020000);
  const __amdgpu_buffer_rsrc_t rcn = __builtin_amdgcn_make_buffer_rsrc((void*)p.cnt, 0, CNT_LINES * 128, 0x00020000);

  bf16x8 wf[2][16];
#pragma unroll
  for (int t = 0; t < 2; ++t) {
    const u16* src = Wqkv_t + (size_t)(t * BHD + h * 128 + 16 * w + fr) * 512 + g * 8;
#pragma unroll
    for (int ks = 0; ks < 16; ++ks) wf[t][ks] = *(const bf16x8*)(src + ks * 32);
  }
  {
#pragma unroll
    for (int i = 0; i < 8; ++i) { const int f = w * 8 + i, j = f >> 4, ks = f & 15;
      *(bf16x8*)(smem + L_WV + (f * 64 + lane) * 16) = *(const bf16x8*)(Wqkv_t + (size_t)(2 * BHD + h * 128 + 64 * a + 16 * j + fr) * 512 + ks * 32 + g * 8); }
#pragma unroll
    for (int i = 0; i < 8; ++i) { const int f = w * 8 + i, nt = f >> 1, ks2 = f & 1;
      *(bf16x8*)(smem + L_WO + (f * 64 + lane) * 16) = *(const bf16x8*)(Wo_t + (size_t)(16 * nt + fr) * BHD + h * 128 + 64 * a + ks2 * 32 + g * 8); }
  }
  for (int c = tid; c < L_VT_BYTES / 16; c += CT) *(u32x4*)(smem + L_VT + c * 16) = u32x4{0u, 0u, 0u, 0u};
  LBAR();

  u32x4 kst, vst;
  const int ktok = tid >> 4, kd8 = tid & 15;
  const int vdim = tid >> 2, vc = tid & 3;
  const int k_voff = (ktok * BN3 + BHD + h * 128 + kd8 * 8) * 2;
  const int v_voff = ((h * 128 + 64 * a + vdim) * 32 + vc * 8) * 2;
  auto kv_load = [&](int s) {
    if ((s & 3) == 0) cnt_wait(rcn, CNT_KV + (s >> 2), 64, rtm);
    kst = __builtin_amdgcn_raw_buffer_load_b128(rqk, k_voff, s * 32 * BN3 * 2, AUX_SC1);
    if (tid < 256) vst = __builtin_amdgcn_raw_buffer_load_b128(rvx, v_voff, s * 32 * 128 * 32 * 2, AUX_SC1);
  };
  auto kv_store = [&]() {
    *(u32x4*)(KALL + (8 + ktok) * KS_LD + kd8 * 8) = kst;
    if (tid < 256) *(u32x4*)(VT + vdim * VT_LDB + 8 + vc * 8) = vst;
  };
  const int km_voff = (fr * BHD + h * 128 + 16 * w + 4 * g) * 2;
  const int vm_voff = ((h * 128 + 64 * a + 16 * w + fr) * 8 + 4 * g) * 2;
  const int ib_voff = (((g >> 1) * 64 + b) * 64 + fr * 8 + 4 * (g & 1)) * 4;
  const int ip_voff = (b * 64 * 64 + tid * 4) * 4;
  const int mp_voff = tid * 16;
  kv_load(0);
  kv_store();
  { const int r = tid >> 6, k = (tid & 63) * 8;
    *(u32x4*)(smem + L_MX + (k >> 3) * 128 + r * 16) = *(const u32x4*)(mem0 + r * 512 + k); }

  const char* mxl = smem + L_MX + g * 128 + (fr & 7) * 16;
  for (int s = 0; s < NSEG; ++s) {
    LBAR();
    if (s + 1 < NSEG) kv_load(s + 1);
    f32x4 acc[2];
#pragma unroll
    for (int t = 0; t < 2; ++t) acc[t] = f32x4{0.f, 0.f, 0.f, 0.f};
    {
      bf16x8 mq[4];
#pragma unroll
      for (int i = 0; i < 4; ++i) mq[i] = *(const bf16x8*)(mxl + i * 512);
#pragma unroll
      for (int ks = 0; ks < 16; ++ks) {
        const bf16x8 mf = mq[ks & 3];
        if (ks + 4 < 16) mq[ks & 3] = *(const bf16x8*)(mxl + (ks + 4) * 512);
        acc[0] = __builtin_amdgcn_mfma_f32_16x16x32_bf16(wf[0][ks], mf, acc[0], 0, 0, 0);
        acc[1] = __builtin_amdgcn_mfma_f32_16x16x32_bf16(wf[1][ks], mf, acc[1], 0, 0, 0);
      }
    }
    STAMP(0);
    {
      const int n0 = 16 * w + 4 * g;
      u32x2 pq, pk2;
      pq[0] = pack2(acc[0][0], acc[0][1]); pq[1] = pack2(acc[0][2], acc[0][3]);
      pk2[0] = pack2(acc[1][0], acc[1][1]); pk2[1] = pack2(acc[1][2], acc[1][3]);
      if (fr < 8) { *(u32x2*)(QS + fr * KS_LD + n0) = pq; *(u32x2*)(KALL + fr * KS_LD + n0) = pk2;
                    if (a == 0) __builtin_amdgcn_raw_buffer_store_b64(pk2, rkm, km_voff, s * 8 * BHD * 2, AUX_SC1); }
    }
    LBAR();
    STAMP(1);
    bf16x8 pf[2];
    if (w < 4) {
      const char* wvb = smem + L_WV + (w * 16 * 64 + lane) * 16;
      f32x4 av = {0.f, 0.f, 0.f, 0.f};
      bf16x8 mfa = *(const bf16x8*)(mxl), mfb = *(const bf16x8*)(mxl + 512);
      bf16x8 wva = *(const bf16x8*)(wvb), wvb2 = *(const bf16x8*)(wvb + 1024);
#pragma unroll
      for (int ks = 0; ks < 16; ++ks) {
        const bf16x8 mf = (ks & 1) ? mfb : mfa, wv = (ks & 1) ? wvb2 : wva;
        if (ks + 2 < 16) {
          if (ks & 1) { mfb = *(const bf16x8*)(mxl + (ks + 2) * 512); wvb2 = *(const bf16x8*)(wvb + (ks + 2) * 1024); }
          else { mfa = *(const bf16x8*)(mxl + (ks + 2) * 512); wva = *(const bf16x8*)(wvb + (ks + 2) * 1024); }
        }
        av = __builtin_amdgcn_mfma_f32_16x16x32_bf16(mf, wv, av, 0, 0, 0);
      }
      u32x2 pv2; pv2[0] = pack2(av[0], av[1]); pv2[1] = pack2(av[2], av[3]);
      if (g < 2) { *(u32x2*)(VT + (16 * w + fr) * VT_LDB + 4 * g) = pv2; __builtin_amdgcn_raw_buffer_store_b64(pv2, rvm, vm_voff, s * 32 * 128 * 8 * 2, AUX_SC1); }
    } else {
      bf16x8 qf[4];
#pragma unroll
      for (int ks = 0; ks < 4; ++ks) qf[ks] = *(const bf16x8*)(QS + (fr & 7) * KS_LD + 32 * ks + 8 * g);
      f32x4 sc[3];
      {
        const u16* kb = KALL + fr * KS_LD + 8 * g;
        bf16x8 kf[3][4];
#pragma unroll
        for (int t = 0; t < 3; ++t)
#pragma unroll
          for (int ks = 0; ks < 4; ++ks) kf[t][ks] = *(const bf16x8*)(kb + 16 * t * KS_LD + 32 * ks);
#pragma unroll
        for (int t = 0; t < 3; ++t) sc[t] = f32x4{0.f, 0.f, 0.f, 0.f};
#pragma unroll
        for (int ks = 0; ks < 4; ++ks)
#pragma unroll
          for (int t = 0; t < 3; ++t) sc[t] = __builtin_amdgcn_mfma_f32_16x16x32_bf16(kf[t][ks], qf[ks], sc[t], 0, 0, 0);
      }
      float pv[3][4]; float mx = -INFINITY;
#pragma unroll
      for (int t = 0; t < 3; ++t)
#pragma unroll
        for (int r = 0; r < 4; r += 2) {
          const unsigned c1 = pack2(sc[t][r], sc[t][r + 1]);
          const unsigned c2 = pack2(__uint_as_float(c1 << 16) * BSCALE, __uint_as_float(c1 & 0xffff0000u) * BSCALE);
          float v0 = __uint_as_float(c2 << 16), v1 = __uint_as_float(c2 & 0xffff0000u);
          if (t == 2 && g >= 2) { v0 = -INFINITY; v1 = -INFINITY; }
          pv[t][r] = v0; pv[t][r + 1] = v1; mx = max3f(mx, v0, v1);
        }
      mx = xmax16(mx); mx = xmax32(mx);
      float sum = 0.f;
#pragma unroll
      for (int t = 0; t < 3; ++t)
#pragma unroll
        for (int r = 0; r < 4; ++r) { pv[t][r] = __expf(pv[t][r] - mx); sum += pv[t][r]; }
      sum = xsum16(sum); sum = xsum32(sum);
      const float inv = 1.0f / sum;
      u32x4 p0, p1;
      p0[0] = pack2(pv[0][0] * inv, pv[0][1] * inv); p0[1] = pack2(pv[0][2] * inv, pv[0][3] * inv);
      p0[2] = pack2(pv[1][0] * inv, pv[1][1] * inv); p0[3] = pack2(pv[1][2] * inv, pv[1][3] * inv);
      p1[0] = pack2(pv[2][0] * inv, pv[2][1] * inv); p1[1] = pack2(pv[2][2] * inv, pv[2][3] * inv);
      p1[2] = 0u; p1[3] = 0u;
      pf[0] = __builtin_bit_cast(bf16x8, p0); pf[1] = __builtin_bit_cast(bf16x8, p1);
    }
    LBAR();
    if (w >= 4) {
      const u16* vrow = VT + (16 * (w & 3) + fr) * VT_LDB + 4 * g;
      const u32x2 v00 = *(const u32x2*)(vrow), v01 = *(const u32x2*)(vrow + 16), v10 = *(const u32x2*)(vrow + 32), v11 = *(const u32x2*)(vrow + 48);
      const u32x4 va0 = {v00[0], v00[1], v01[0], v01[1]}, va1 = {v10[0], v10[1], v11[0], v11[1]};
      f32x4 oa = {0.f, 0.f, 0.f, 0.f};
      oa = __builtin_amdgcn_mfma_f32_16x16x32_bf16(__builtin_bit_cast(bf16x8, va0), pf[0], oa, 0, 0, 0);
      oa = __builtin_amdgcn_mfma_f32_16x16x32_bf16(__builtin_bit_cast(bf16x8, va1), pf[1], oa, 0, 0, 0);
      u32x2 pk; pk[0] = pack2(oa[0], oa[1]); pk[1] = pack2(oa[2], oa[3]);
      if (fr < 8) *(u32x2*)(OS + fr * OS_LD + 16 * (w & 3) + 4 * g) = pk;
    }
    bf16x8 wo[8];
    {
      const char* wob = smem + L_WO + (w * 8 * 64 + lane) * 16;
#pragma unroll
      for (int i = 0; i < 8; ++i) wo[i] = *(const bf16x8*)(wob + i * 1024);
    }
    LBAR();
    STAMP(2);
    if (s + 1 == NSEG) { VMDRAIN(); LBAR(); if (tid == 0) cnt_add(p.cnt + (CNT_SEG + s) * 32); break; }
    const int slot = s & 1;
    {
      bf16x8 of[2];
#pragma unroll
      for (int ks = 0; ks < 2; ++ks) of[ks] = *(const bf16x8*)(OS + (fr & 7) * OS_LD + 32 * ks + 8 * g);
#pragma unroll
      for (int i = 0; i < 4; ++i) {
        f32x4 acc2 = {0.f, 0.f, 0.f, 0.f};
        acc2 = __builtin_amdgcn_mfma_f32_16x16x32_bf16(wo[2 * i], of[0], acc2, 0, 0, 0);
        acc2 = __builtin_amdgcn_mfma_f32_16x16x32_bf16(wo[2 * i + 1], of[1], acc2, 0, 0, 0);
        u32x4 st;
#pragma unroll
        for (int r = 0; r < 4; ++r) { const unsigned ub = __float_as_uint(acc2[r]); st[r] = ub == SENT ? 0x7FC00000u : ub; }
        if (fr < 8) __builtin_amdgcn_raw_buffer_store_b128(st, rin, ib_voff, (slot * INBOX_DW + (4 * w + i) * 2 * 64 * 64) * 4, AUX_SC1);
      }
    }
    STAMP(3);
    kv_store();
    STAMP(4);
#pragma unroll
    for (int i = 0; i < CH_DELAY1; ++i) __builtin_amdgcn_s_sleep(4);
    u32x4 pib[2];
    {
      unsigned spins = 0;
      for (;;) {
        bool ok = true;
#pragma unroll
        for (int i = 0; i < 2; ++i) { pib[i] = __builtin_amdgcn_raw_buffer_load_b128(rin, ip_voff, (slot * INBOX_DW + CT * i * 4) * 4, AUX_SC1); ok &= valid4(pib[i]); }
        if (__all(ok)) break;
        if ((++spins & 63u) == 0u) {
          const unsigned t0 = __builtin_amdgcn_readfirstlane(__builtin_amdgcn_raw_buffer_load_b32(rtm, 0, 0, AUX_SC1));
          if (spins > SPIN_LIMIT || t0 == 1u) { if (lane == 0) __builtin_amdgcn_raw_buffer_store_b32(1u, rtm, 0, 0, AUX_SC1); break; }
        }
        __builtin_amdgcn_s_sleep(2);
      }
    }
    STAMP(5);
    {
      f32x4 ps;
#pragma unroll
      for (int r = 0; r < 4; ++r) ps[r] = xsum32(xsum16(__uint_as_float(pib[0][r]) + __uint_as_float(pib[1][r])));
      if (lane < 16) *(f32x4*)(smem + L_RS + (w * 16 + lane) * 16) = ps;
    }
    LBAR();
    if (tid == 64) cnt_add(p.cnt + (CNT_SEG + s) * 32);
    if (tid < 16) {
      f32x4 t4 = *(const f32x4*)(smem + L_RS + tid * 16);
#pragma unroll
      for (int j = 1; j < 8; ++j) { const f32x4 o4 = *(const f32x4*)(smem + L_RS + (j * 16 + tid) * 16); t4 = t4 + o4; }
      u32x2 pk; pk[0] = canon(pack2(t4[0], t4[1])); pk[1] = canon(pack2(t4[2], t4[3]));
      __builtin_amdgcn_raw_buffer_store_b64(pk, rmx, (b * 32 + tid * 2) * 4, (s + 1) * MEMX_SLOT_DW * 4, AUX_SC1);
    }
    {
      const u32x4 sent = {SENT, SENT, SENT, SENT};
#pragma unroll
      for (int i = 0; i < 2; ++i) __builtin_amdgcn_raw_buffer_store_b128(sent, rin, ip_voff, (slot * INBOX_DW + CT * i * 4) * 4, AUX_SC1);
    }
    STAMP(6);
#pragma unroll
    for (int i = 0; i < CH_DELAY2; ++i) __builtin_amdgcn_s_sleep(4);
    {
      u32x4 m;
      unsigned spins = 0;
      for (;;) {
        m = __builtin_amdgcn_raw_buffer_load_b128(rmx, mp_voff, (s + 1) * MEMX_SLOT_DW * 4, AUX_SC1);
        if (__all(valid4(m))) break;
        if ((++spins & 63u) == 0u) {
          const unsigned t0 = __builtin_amdgcn_readfirstlane(__builtin_amdgcn_raw_buffer_load_b32(rtm, 0, 0, AUX_SC1));
          if (spins > SPIN_LIMIT || t0 == 1u) { if (lane == 0) __builtin_amdgcn_raw_buffer_store_b32(1u, rtm, 0, 0, AUX_SC1); break; }
        }
        __builtin_amdgcn_s_sleep(2);
      }
      *(u32x4*)(smem + L_MX + tid * 16) = m;
    }
    STAMP(7);
  }
}

extern "C" void kernel_launch(void* const* d_in, const int* in_sizes, int n_in,
                              void* d_out, int out_size, void* d_ws, size_t ws_size,
                              hipStream_t stream) {
  const u16* A_x = (const u16*)d_in[0];
  const u16* A_wq = (const u16*)d_in[1];
  const u16* A_wk = (const u16*)d_in[2];
  const u16* A_wv = (const u16*)d_in[3];
  const u16* A_wo = (const u16*)d_in[4];
  const u16* B_mem0 = (const u16*)d_in[6];
  const u16* B_Wq = (const u16*)d_in[7];
  const u16* B_Wk = (const u16*)d_in[8];
  const u16* B_Wv = (const u16*)d_in[9];
  const u16* B_Wo = (const u16*)d_in[10];
  u16* out = (u16*)d_out;

  char* ws = (char*)d_ws; size_t off = 0;
  auto alloc = [&](size_t bytes) { char* p = ws + off; off += (bytes + 255) & ~(size_t)255; return (u16*)p; };
  u16* xn = alloc((size_t)S_ * D_ * 2);
  u16* x1 = alloc((size_t)S_ * D_ * 2);
  u16* x2 = alloc((size_t)S_ * D_ * 2);
  u16* x3 = alloc((size_t)S_ * D_ * 2);
  u16* aqkv = alloc((size_t)S_ * AQ_LD * 2);
  u16* ao = alloc((size_t)S_ * D_ * 2);
  u16* Awqkv_t = alloc((size_t)AQ_LD * D_ * 2);
  u16* Awo_t = alloc((size_t)D_ * D_ * 2);
  u16* BWqkv_t = alloc((size_t)BN3 * D_ * 2);
  u16* BWo_t = alloc((size_t)D_ * BHD * 2);
  u16* qkvx = alloc((size_t)S_ * BN3 * 2);
  u16* ox = alloc((size_t)S_ * BHD * 2);
  u16* vtx = alloc((size_t)S_ * BHD * 2);
  u16* akvx = alloc((size_t)S_ * 32 * 2);
  u16* kmall = alloc((size_t)NSEG * 8 * BHD * 2);
  u16* vmall = alloc((size_t)NSEG * 8 * BHD * 2);
  char* exch0 = ws + off;
  unsigned* tmo = (unsigned*)alloc(256 + 64 * 8 * 8 * 8);
  unsigned* memx = (unsigned*)alloc((size_t)2 * (NSEG + 1) * MEMX_SLOT_DW * 4);
  unsigned* inbox = (unsigned*)alloc((size_t)2 * INBOX_DW * 4);
  unsigned* cnts = (unsigned*)alloc((size_t)2 * CNT_LINES * 128);
  hipMemsetAsync(exch0, 0xFF, (size_t)((ws + off) - exch0), stream);
  int blayer = 0;

  {
    TrJobs jb;
    const u16* srcs[8] = {A_wq, A_wk, A_wv, A_wo, B_Wq, B_Wk, B_Wv, B_Wo};
    u16* dsts[8] = {Awqkv_t, Awqkv_t + 512 * 512, Awqkv_t + 528 * 512, Awo_t, BWqkv_t, BWqkv_t + (size_t)4096 * 512, BWqkv_t + (size_t)8192 * 512, BWo_t};
    const int Ks[8] = {512, 512, 512, 512, 512, 512, 512, 4096}, Ns[8] = {512, 16, 16, 512, 4096, 4096, 4096, 512};
    int t0 = 0;
    for (int i = 0; i < 8; ++i) { jb.src[i] = srcs[i]; jb.dst[i] = dsts[i]; jb.K[i] = Ks[i]; jb.N[i] = Ns[i]; jb.tile0[i] = t0; t0 += (Ks[i] / 64) * ((Ns[i] + 63) / 64); }
    jb.tile0[8] = t0; jb.pad_ = 0;
    transpose_all_kernel<<<t0, 256, 0, stream>>>(jb);
  }

  float* part = (float*)alloc((size_t)4 * S_ * D_ * 4);
  auto layerA = [&](const u16* xin, u16* xout, bool ln_next) {
    gemm_nt<64, 64, 3><<<dim3(9, 32), 256, 0, stream>>>(xn, 512, Awqkv_t, 512, aqkv, AQ_LD, AQ_LD, 512, nullptr, akvx);
    attnA_kernel<<<256, 512, 0, stream>>>(aqkv, akvx, ao);
    gemm_nt<64, 64, 4><<<dim3(8, 32, 1), 256, 0, stream>>>(ao, 512, Awo_t, 512, nullptr, 512, 512, 512, nullptr, (u16*)part);
    finish_ln_kernel<1><<<512, 256, 0, stream>>>(part, xin, xout, ln_next ? xn : nullptr);
  };
  auto layerB = [&](const u16* xin, u16* xout, bool ln_next) {
    BArgs ba;
    ba.xn = xn; ba.Wqkv_t = BWqkv_t; ba.Wo_t = BWo_t; ba.mem0 = B_mem0;
    ba.qkvx = qkvx; ba.vtx = vtx; ba.kmall = kmall; ba.vmT = vmall; ba.ox = ox; ba.part = part;
    ba.memx = memx + (size_t)blayer * (NSEG + 1) * MEMX_SLOT_DW; ba.inbox = inbox; ba.tmo = tmo; ba.cnt = cnts + blayer * CNT_LINES * 32;
    blayer_kernel<<<NCH + NHELP, CT, 0, stream>>>(ba);
    ++blayer;
    finish_ln_kernel<4><<<512, 256, 0, stream>>>(part, xin, xout, ln_next ? xn : nullptr);
  };
  ln_kernel<<<512, 256, 0, stream>>>(A_x, xn);
  layerA(A_x, x1, true);
  layerB(x1, x2, true);
  layerA(x2, x3, true);
  layerB(x3, out, false);
}
```

```cpp
#include <hip/hip_runtime.h>
#include <hip/hip_bf16.h>
#include <stdint.h>

typedef unsigned short u16;
using bf16x8 = __attribute__((ext_vector_type(8))) short;
using f32x4  = __attribute__((ext_vector_type(4))) float;
using f32x16 = __attribute__((ext_vector_type(16))) float;
using u32x4  = __attribute__((ext_vector_type(4))) unsigned;
using u32x2  = __attribute__((ext_vector_type(2))) unsigned;
#define DI __device__ __forceinline__

DI float bf2f(u16 x) { return __uint_as_float(((unsigned)x) << 16); }
DI u16 f2bf(float f) { __bf16 b = (__bf16)f; return __builtin_bit_cast(u16, b); }
DI float rbf(float f) { return bf2f(f2bf(f)); }
typedef __attribute__((ext_vector_type(2))) __bf16 bf16x2_t;
typedef __attribute__((ext_vector_type(2))) float f32x2_t;
DI unsigned cvtpk(float lo, float hi) { const f32x2_t v = {lo, hi}; return __builtin_bit_cast(unsigned, __builtin_convertvector(v, bf16x2_t)); }
DI unsigned pack2(float lo, float hi) { return cvtpk(lo, hi); }
DI float max3f(float a, float b, float c) { float r; asm("v_max3_f32 %0, %1, %2, %3" : "=v"(r) : "v"(a), "v"(b), "v"(c)); return r; }
DI float max3f_pad(float a, float b, float c) { float r; asm("v_max3_f32 %0, %1, %2, %3\n\ts_nop 1" : "=v"(r) : "v"(a), "v"(b), "v"(c)); return r; }
DI float xmax16(float x) { x = max3f_pad(x, x, x); auto r = __builtin_amdgcn_permlane16_swap(__float_as_uint(x), __float_as_uint(x), false, false); return max3f(__uint_as_float(r[0]), __uint_as_float(r[1]), __uint_as_float(r[1])); }
DI float xmax32(float x) { x = max3f_pad(x, x, x); auto r = __builtin_amdgcn_permlane32_swap(__float_as_uint(x), __float_as_uint(x), false, false); return max3f(__uint_as_float(r[0]), __uint_as_float(r[1]), __uint_as_float(r[1])); }
DI float xsum16(float x) { auto r = __builtin_amdgcn_permlane16_swap(__float_as_uint(x), __float_as_uint(x), false, false); return __uint_as_float(r[0]) + __uint_as_float(r[1]); }
DI float xsum32(float x) { auto r = __builtin_amdgcn_permlane32_swap(__float_as_uint(x), __float_as_uint(x), false, false); return __uint_as_float(r[0]) + __uint_as_float(r[1]); }

constexpr int S_ = 2048, D_ = 512;
constexpr int AH = 32, ADH = 16;
constexpr int BH = 32, BDH = 128, BM_ = 8, SEG = 32, NSEG = 64;
constexpr int BN3 = 3 * BH * BDH;
constexpr int BHD = BH * BDH;

__global__ __launch_bounds__(256) void ln_kernel(const u16* __restrict__ x, u16* __restrict__ y) {
  const int row = blockIdx.x * 4 + (threadIdx.x >> 6), lane = threadIdx.x & 63;
  const u32x4 v = *(const u32x4*)(x + (size_t)row * D_ + lane * 8);
  float f[8];
#pragma unroll
  for (int i = 0; i < 4; ++i) { f[2 * i] = __uint_as_float(v[i] << 16); f[2 * i + 1] = __uint_as_float(v[i] & 0xffff0000u); }
  float s = 0.f;
#pragma unroll
  for (int i = 0; i < 8; ++i) s += f[i];
#pragma unroll
  for (int o = 32; o >= 1; o >>= 1) s += __shfl_xor(s, o);
  const float mean = s * (1.0f / D_);
  float q = 0.f;
#pragma unroll
  for (int i = 0; i < 8; ++i) { const float d = f[i] - mean; q += d * d; }
#pragma unroll
  for (int o = 32; o >= 1; o >>= 1) q += __shfl_xor(q, o);
  const float var = q * (1.0f / D_);
  const float mb = rbf(mean), vb = rbf(var);
  const float den = rbf(sqrtf(rbf(vb + 1e-5f)));
  u32x4 o;
#pragma unroll
  for (int i = 0; i < 4; ++i) o[i] = pack2(rbf(f[2 * i] - mb) / den, rbf(f[2 * i + 1] - mb) / den);
  *(u32x4*)(y + (size_t)row * D_ + lane * 8) = o;
}

template <int NP>
__global__ __launch_bounds__(256) void finish_ln_kernel(const float* __restrict__ P, const u16* __restrict__ resid, u16* __restrict__ xo, u16* __restrict__ y) {
  const int row = blockIdx.x * 4 + (threadIdx.x >> 6), lane = threadIdx.x & 63;
  float f[8];
  {
    f32x4 a0 = *(const f32x4*)(P + (size_t)row * D_ + lane * 8), a1 = *(const f32x4*)(P + (size_t)row * D_ + lane * 8 + 4);
#pragma unroll
    for (int z = 1; z < NP; ++z) {
      const f32x4 b0 = *(const f32x4*)(P + (size_t)z * S_ * D_ + (size_t)row * D_ + lane * 8), b1 = *(const f32x4*)(P + (size_t)z * S_ * D_ + (size_t)row * D_ + lane * 8 + 4);
      a0 = a0 + b0; a1 = a1 + b1;
    }
    const u32x4 rv = *(const u32x4*)(resid + (size_t)row * D_ + lane * 8);
#pragma unroll
    for (int i = 0; i < 4; ++i) {
      const unsigned t = i < 2 ? cvtpk(a0[2 * i], a0[2 * i + 1]) : cvtpk(a1[2 * i - 4], a1[2 * i - 3]);
      const unsigned xs = cvtpk(__uint_as_float(t << 16) + __uint_as_float(rv[i] << 16), __uint_as_float(t & 0xffff0000u) + __uint_as_float(rv[i] & 0xffff0000u));
      f[2 * i] = __uint_as_float(xs << 16); f[2 * i + 1] = __uint_as_float(xs & 0xffff0000u);
    }
    u32x4 o;
#pragma unroll
    for (int i = 0; i < 4; ++i) o[i] = cvtpk(f[2 * i], f[2 * i + 1]);
    *(u32x4*)(xo + (size_t)row * D_ + lane * 8) = o;
  }
  if (y == nullptr) return;
  float s = 0.f;
#pragma unroll
  for (int i = 0; i < 8; ++i) s += f[i];
#pragma unroll
  for (int o = 32; o >= 1; o >>= 1) s += __shfl_xor(s, o);
  const float mean = s * (1.0f / D_);
  float q = 0.f;
#pragma unroll
  for (int i = 0; i < 8; ++i) { const float d = f[i] - mean; q += d * d; }
#pragma unroll
  for (int o = 32; o >= 1; o >>= 1) q += __shfl_xor(q, o);
  const float var = q * (1.0f / D_);
  const float mb = rbf(mean), vb = rbf(var);
  const float den = rbf(sqrtf(rbf(vb + 1e-5f)));
  u32x4 o;
#pragma unroll
  for (int i = 0; i < 4; ++i) o[i] = pack2(rbf(f[2 * i] - mb) / den, rbf(f[2 * i + 1] - mb) / den);
  *(u32x4*)(y + (size_t)row * D_ + lane * 8) = o;
}

struct TrJobs { const u16* src[8]; u16* dst[8]; int K[8]; int N[8]; int tile0[9]; int pad_; };
__global__ __launch_bounds__(256) void transpose_all_kernel(TrJobs jobs) {
  __shared__ __attribute__((aligned(16))) u16 t[64][68];
  int j = 0;
#pragma unroll
  for (int i = 1; i < 8; ++i) j += (int)blockIdx.x >= jobs.tile0[i];
  const int K = jobs.K[j], N = jobs.N[j], tl = blockIdx.x - jobs.tile0[j];
  const int ntn = (N + 63) >> 6, n0 = (tl % ntn) * 64, k0 = (tl / ntn) * 64;
  const u16* __restrict__ in = jobs.src[j]; u16* __restrict__ out = jobs.dst[j];
  const int tx = threadIdx.x & 15, ty = threadIdx.x >> 4;
#pragma unroll
  for (int i = 0; i < 4; ++i) {
    const int r = ty + 16 * i, c = tx * 4;
    if (n0 + c < N) {
      const u32x2 v = *(const u32x2*)(in + (size_t)(k0 + r) * N + n0 + c);
      t[c][r] = (u16)(v[0] & 0xffff); t[c + 1][r] = (u16)(v[0] >> 16); t[c + 2][r] = (u16)(v[1] & 0xffff); t[c + 3][r] = (u16)(v[1] >> 16);
    }
  }
  __syncthreads();
#pragma unroll
  for (int i = 0; i < 4; ++i) {
    const int n = ty + 16 * i;
    if (n0 + n < N) *(u32x2*)(out + (size_t)(n0 + n) * K + k0 + tx * 4) = *(const u32x2*)(&t[n][tx * 4]);
  }
}

template <int BM, int BN, int EPI>
__global__ __launch_bounds__(256) void gemm_nt(const u16* __restrict__ A, int lda, const u16* __restrict__ Bt, int ldb,
                                               u16* __restrict__ C, int ldc, int N, int K, const u16* __restrict__ R, u16* __restrict__ X) {
  constexpr int BK = 64, LS = 72;
  constexpr int MT = BM / 32, NT = BN / 32;
  constexpr int CA = BM * 8 / 256, CB = BN * 8 / 256;
  constexpr int LDS_AB = (BM + BN) * LS * 2, LDS_C = EPI == 4 ? BM * (BN + 4) * 4 : BM * (BN + 8) * 2;
  __shared__ __attribute__((aligned(16))) char smem[LDS_AB > LDS_C ? LDS_AB : LDS_C];
  u16* As = (u16*)smem; u16* Bs = As + BM * LS;
  const int tid = threadIdx.x, lane = tid & 63, w = tid >> 6, wm = w >> 1, wn = w & 1;
  const int m0 = blockIdx.y * BM, n0 = blockIdx.x * BN;
  const int fr = lane & 15, fq = lane >> 4;
  if (EPI == 4) { A += (size_t)blockIdx.z * K; Bt += (size_t)blockIdx.z * K; }
  f32x4 acc[MT][NT];
#pragma unroll
  for (int i = 0; i < MT; ++i)
#pragma unroll
    for (int j = 0; j < NT; ++j) acc[i][j] = f32x4{0.f, 0.f, 0.f, 0.f};
  u32x4 ra[CA], rb[CB];
  auto gload = [&](int k0) {
#pragma unroll
    for (int i = 0; i < CA; ++i) { const int c = tid + i * 256, r = c >> 3, c8 = c & 7; ra[i] = *(const u32x4*)(A + (size_t)(m0 + r) * lda + k0 + c8 * 8); }
#pragma unroll
    for (int i = 0; i < CB; ++i) { const int c = tid + i * 256, r = c >> 3, c8 = c & 7; int n = n0 + r; n = n < N ? n : N - 1;
      rb[i] = *(const u32x4*)(Bt + (size_t)n * ldb + k0 + c8 * 8); }
  };
  gload(0);
  for (int k0 = 0; k0 < K; k0 += BK) {
    __syncthreads();
#pragma unroll
    for (int i = 0; i < CA; ++i) { const int c = tid + i * 256, r = c >> 3, c8 = c & 7; *(u32x4*)(As + r * LS + c8 * 8) = ra[i]; }
#pragma unroll
    for (int i = 0; i < CB; ++i) { const int c = tid + i * 256, r = c >> 3, c8 = c & 7; *(u32x4*)(Bs + r * LS + c8 * 8) = rb[i]; }
    __syncthreads();
    if (k0 + BK < K) gload(k0 + BK);
#pragma unroll
    for (int kk = 0; kk < 2; ++kk) {
      bf16x8 af[MT], bfr[NT];
#pragma unroll
      for (int i = 0; i < MT; ++i) af[i] = *(const bf16x8*)(As + (wm * (BM / 2) + i * 16 + fr) * LS + kk * 32 + fq * 8);
#pragma unroll
      for (int j = 0; j < NT; ++j) bfr[j] = *(const bf16x8*)(Bs + (wn * (BN / 2) + j * 16 + fr) * LS + kk * 32 + fq * 8);
#pragma unroll
      for (int i = 0; i < MT; ++i)
#pragma unroll
        for (int j = 0; j < NT; ++j) acc[i][j] = __builtin_amdgcn_mfma_f32_16x16x32_bf16(af[i], bfr[j], acc[i][j], 0, 0, 0);
    }
  }
  if ((EPI == 2 && n0 >= 2 * BHD) || (EPI == 3 && n0 + BN > 512)) {
#pragma unroll
    for (int i = 0; i < MT; ++i)
#pragma unroll
      for (int j = 0; j < NT; ++j) {
        const int n = n0 + wn * (BN / 2) + j * 16 + fr, m = m0 + wm * (BM / 2) + i * 16 + fq * 4;
        u32x2 pk; pk[0] = pack2(acc[i][j][0], acc[i][j][1]); pk[1] = pack2(acc[i][j][2], acc[i][j][3]);
        if (EPI == 2) {
          const int hh = (n - 2 * BHD) >> 7, dim = n & 127;
          *(u32x2*)(X + ((size_t)((m >> 5) * 32 + hh) * 128 + dim) * 32 + (m & 31)) = pk;
        } else if (n >= 512 && n < 528) {
#pragma unroll
          for (int r = 0; r < 4; ++r) X[(size_t)(m + r) * 16 + (n - 512)] = (u16)(r & 1 ? pk[r >> 1] >> 16 : pk[r >> 1] & 0xffff);
        } else if (n >= 528 && n < 544) {
          const int pos = (m & ~15) | ((fq & 1) * 8 + (fq >> 1) * 4);
          *(u32x2*)(X + 2048 * 16 + (size_t)(n - 528) * 2048 + pos) = pk;
        } else if (n < 512) {
#pragma unroll
          for (int r = 0; r < 4; ++r) C[(size_t)(m + r) * ldc + n] = (u16)(r & 1 ? pk[r >> 1] >> 16 : pk[r >> 1] & 0xffff);
        }
      }
    return;
  }
  if (EPI == 4) {
    __syncthreads();
    constexpr int FS = BN + 4;
    float* Fs = (float*)smem;
#pragma unroll
    for (int i = 0; i < MT; ++i)
#pragma unroll
      for (int j = 0; j < NT; ++j)
#pragma unroll
        for (int r = 0; r < 4; ++r) Fs[(wm * (BM / 2) + i * 16 + fq * 4 + r) * FS + wn * (BN / 2) + j * 16 + fr] = acc[i][j][r];
    __syncthreads();
    float* Pz = (float*)X + (size_t)blockIdx.z * S_ * ldc;
    for (int c = tid; c < BM * BN / 4; c += 256) { const int r = c / (BN / 4), c4 = c % (BN / 4);
      *(f32x4*)(Pz + (size_t)(m0 + r) * ldc + n0 + c4 * 4) = *(const f32x4*)(Fs + r * FS + c4 * 4); }
    return;
  }
  __syncthreads();
  constexpr int CS = BN + 8;
  u16* Cs = (u16*)smem;
#pragma unroll
  for (int i = 0; i < MT; ++i)
#pragma unroll
    for (int j = 0; j < NT; ++j)
#pragma unroll
      for (int r = 0; r < 4; ++r) Cs[(wm * (BM / 2) + i * 16 + fq * 4 + r) * CS + wn * (BN / 2) + j * 16 + fr] = f2bf(acc[i][j][r]);
  __syncthreads();
  for (int c = tid; c < BM * BN / 8; c += 256) {
    const int r = c / (BN / 8), c8 = c % (BN / 8);
    const int n = n0 + c8 * 8;
    if (n >= N) continue;
    u32x4 v = *(const u32x4*)(Cs + r * CS + c8 * 8);
    if (EPI == 1) {
      const u32x4 rr = *(const u32x4*)(R + (size_t)(m0 + r) * ldc + n);
#pragma unroll
      for (int i = 0; i < 4; ++i) {
        const float lo = bf2f((u16)(v[i] & 0xffff)) + bf2f((u16)(rr[i] & 0xffff));
        const float hi = bf2f((u16)(v[i] >> 16)) + bf2f((u16)(rr[i] >> 16));
        v[i] = pack2(lo, hi);
      }
    }
    *(u32x4*)(C + (size_t)(m0 + r) * ldc + n) = v;
  }
}

constexpr int AQ_LD = 544;
constexpr int VT_LD = 2048 + 8;
__global__ __launch_bounds__(512) void attnA_kernel(const u16* __restrict__ qkv, const u16* __restrict__ kvx, u16* __restrict__ o) {
  __shared__ __attribute__((aligned(16))) u16 Ks[2048 * 16];
  __shared__ __attribute__((aligned(16))) u16 Vt[17 * VT_LD];
  const int tid = threadIdx.x, lane = tid & 63, w = tid >> 6;
  for (int c = tid; c < 2048 * 2; c += 512) *(u32x4*)(Ks + c * 8) = *(const u32x4*)(kvx + c * 8);
  for (int c = tid; c < 16 * 256; c += 512) { const int d = c >> 8, k8 = c & 255; *(u32x4*)(Vt + d * VT_LD + k8 * 8) = *(const u32x4*)(kvx + 2048 * 16 + d * 2048 + k8 * 8); }
  for (int c = tid; c < 256; c += 512) *(u32x4*)(Vt + 16 * VT_LD + c * 8) = u32x4{0x3F803F80u, 0x3F803F80u, 0x3F803F80u, 0x3F803F80u};
  __syncthreads();
  const int tok = blockIdx.x * 8 + w;
  const int c32 = lane & 31, hh = lane >> 5;
  const bf16x8 qb = *(const bf16x8*)(qkv + (size_t)tok * AQ_LD + c32 * 16 + hh * 8);
  const float cs = 0.25f * 1.4426950408889634f;
  float m_run = -INFINITY;
  f32x16 oacc;
#pragma unroll
  for (int i = 0; i < 16; ++i) oacc[i] = 0.f;
  const f32x16 zero16 = oacc;
  const u16* kp = Ks + c32 * 16 + hh * 8;
  const u16* vp = Vt + ((lane & 31) < 16 ? (lane & 15) : 16) * VT_LD + hh * 8;
  auto tile = [&](f32x16& x, int t) {
    float mt = max3f(x[0], x[1], x[2]);
#pragma unroll
    for (int i = 3; i < 15; i += 2) mt = max3f(mt, x[i], x[i + 1]);
    mt = max3f(mt, x[15], x[15]);
    mt = xmax32(mt);
    const float m_new = max3f(m_run, mt, mt);
    const float alpha = __builtin_amdgcn_exp2f((m_run - m_new) * cs);
    const float mc = m_new * cs;
    u32x4 pk0, pk1;
#pragma unroll
    for (int i = 0; i < 16; i += 2) {
      const unsigned pk = cvtpk(__builtin_amdgcn_exp2f(__builtin_fmaf(x[i], cs, -mc)), __builtin_amdgcn_exp2f(__builtin_fmaf(x[i + 1], cs, -mc)));
      if (i < 8) pk0[i >> 1] = pk; else pk1[(i - 8) >> 1] = pk;
    }
    m_run = m_new;
#pragma unroll
    for (int i = 0; i < 9; ++i) oacc[i] *= alpha;
    const bf16x8 va0 = *(const bf16x8*)(vp + t * 32), va1 = *(const bf16x8*)(vp + t * 32 + 16);
    oacc = __builtin_amdgcn_mfma_f32_32x32x16_bf16(va0, __builtin_bit_cast(bf16x8, pk0), oacc, 0, 0, 0);
    oacc = __builtin_amdgcn_mfma_f32_32x32x16_bf16(va1, __builtin_bit_cast(bf16x8, pk1), oacc, 0, 0, 0);
  };
  f32x16 xa = __builtin_amdgcn_mfma_f32_32x32x16_bf16(*(const bf16x8*)(kp), qb, zero16, 0, 0, 0), xb;
  for (int t = 0; t < 64; t += 2) {
    xb = __builtin_amdgcn_mfma_f32_32x32x16_bf16(*(const bf16x8*)(kp + (t + 1) * 32 * 16), qb, zero16, 0, 0, 0);
    tile(xa, t);
    if (t + 2 < 64) xa = __builtin_amdgcn_mfma_f32_32x32x16_bf16(*(const bf16x8*)(kp + (t + 2) * 32 * 16), qb, zero16, 0, 0, 0);
    tile(xb, t + 1);
  }
  const float inv = 1.0f / oacc[8];
  u32x2 o0, o1;
  o0[0] = pack2(oacc[0] * inv, oacc[1] * inv); o0[1] = pack2(oacc[2] * inv, oacc[3] * inv);
  o1[0] = pack2(oacc[4] * inv, oacc[5] * inv); o1[1] = pack2(oacc[6] * inv, oacc[7] * inv);
  u16* op = o + (size_t)tok * 512 + c32 * 16;
  *(u32x2*)(op + 4 * hh) = o0;
  *(u32x2*)(op + 8 + 4 * hh) = o1;
}

DI float red16_max(float v) { v = fmaxf(v, __shfl_xor(v, 1)); v = fmaxf(v, __shfl_xor(v, 2)); v = fmaxf(v, __shfl_xor(v, 4)); v = fmaxf(v, __shfl_xor(v, 8)); return v; }
DI float red16_sum(float v) { v += __shfl_xor(v, 1); v += __shfl_xor(v, 2); v += __shfl_xor(v, 4); v += __shfl_xor(v, 8); return v; }

constexpr float BSCALE = 0.08838834764831845f;
constexpr float NEGBIG = -1000000000.0f;

constexpr unsigned SENT = 0xFFFFFFFFu;
#define AUX_SC1 16
constexpr int KS_LD = 136, VT_LDB = 40, OS_LD = 72;
constexpr int NCH = 64;
constexpr int L_WV = 0, L_WV_BYTES = 4 * 16 * 1024;
constexpr int L_WO = L_WV + L_WV_BYTES, L_WO_BYTES = 32 * 2 * 1024;
constexpr int L_VT = L_WO + L_WO_BYTES, L_VT_BYTES = 64 * VT_LDB * 2;
constexpr int L_K = L_VT + L_VT_BYTES, L_K_BYTES = 40 * KS_LD * 2;
constexpr int L_Q = L_K + L_K_BYTES, L_Q_BYTES = 8 * KS_LD * 2;
constexpr int L_MX = L_Q + L_Q_BYTES, L_MX_BYTES = 8192;
constexpr int L_O = L_MX;
constexpr int L_RS = L_MX + L_MX_BYTES;
constexpr int L_TOTAL = L_RS + 2048;
constexpr int MEMX_SLOT_DW = 64 * 32;
constexpr int INBOX_DW = 64 * 64 * 64;
constexpr unsigned SPIN_LIMIT = 1u << 15;
#define CH_DELAY1 0
#define CH_DELAY2 4

#define STAMP(i)
DI bool valid4(u32x4 v) { return v[0] != SENT && v[1] != SENT && v[2] != SENT && v[3] != SENT; }
DI unsigned canon(unsigned x) { return x == SENT ? 0x7FC07FC0u : x; }

#define LBAR() asm volatile("s_waitcnt lgkmcnt(0)\n\ts_barrier" ::: "memory")
constexpr int CT = 512;

constexpr int CNT_KV = 0, CNT_Q = 16, CNT_SEG = 32, CNT_O = 96, CNT_LINES = 112;
struct BArgs {
  const u16* xn; const u16* Wqkv_t; const u16* Wo_t; const u16* mem0;
  u16* qkvx; u16* vtx; u16* kmall; u16* vmT; u16* ox; float* part;
  unsigned* memx; unsigned* inbox; unsigned* tmo; unsigned* cnt;
};
DI void cnt_add(unsigned* c) { __hip_atomic_fetch_add(c, 1u, __ATOMIC_RELAXED, __HIP_MEMORY_SCOPE_AGENT); }
DI void cnt_wait(__amdgpu_buffer_rsrc_t rc, int line, unsigned n, __amdgpu_buffer_rsrc_t rtm) {
  for (unsigned spins = 0;;) {
    const unsigned v = __builtin_amdgcn_readfirstlane(__builtin_amdgcn_raw_buffer_load_b32(rc, line * 128, 0, AUX_SC1));
    if (v + 1u >= n) return;
    if ((++spins & 15u) == 0u) {
      const unsigned t0 = __builtin_amdgcn_readfirstlane(__builtin_amdgcn_raw_buffer_load_b32(rtm, 0, 0, AUX_SC1));
      if (spins > (SPIN_LIMIT << 2) || t0 == 1u) { __builtin_amdgcn_raw_buffer_store_b32(1u, rtm, 0, 0, AUX_SC1); return; }
    }
    if (spins < 4) __builtin_amdgcn_s_sleep(8); else __builtin_amdgcn_s_sleep(48);
  }
}
#define VMDRAIN() asm volatile("s_waitcnt vmcnt(0)" ::: "memory")

template <int BN, int MODE>
DI void helper_gemm_tile(const BArgs& p, char* smem, int m0, int n0, int kz, int tid) {
  constexpr int BM = 128, LS = 72;
  constexpr int WGN = BN / 32;
  constexpr int WGM = 8 / WGN, WM = BM / WGM;
  constexpr int MT = WM / 16, NT = 2;
  constexpr int K = MODE == 0 ? 512 : 1024;
  constexpr int CA = BM * 8 / CT, CB = (BN * 8 + CT - 1) / CT;
  u16* As = (u16*)smem; u16* Bs = As + BM * LS;
  const int lane = tid & 63, w = tid >> 6, wm = w / WGN, wn = w % WGN, fr = lane & 15, fq = lane >> 4;
  const u16* __restrict__ Bt = (MODE == 0 ? p.Wqkv_t : p.Wo_t) + (MODE == 1 ? kz * K : 0);
  constexpr int ldb = MODE == 0 ? 512 : BHD;
  const __amdgpu_buffer_rsrc_t rox = __builtin_amdgcn_make_buffer_rsrc((void*)p.ox, 0, S_ * BHD * 2, 0x00020000);
  f32x4 acc[MT][NT];
#pragma unroll
  for (int i = 0; i < MT; ++i)
#pragma unroll
    for (int j = 0; j < NT; ++j) acc[i][j] = f32x4{0.f, 0.f, 0.f, 0.f};
  u32x4 ra[CA], rb[CB];
  auto gload = [&](int k0) {
#pragma unroll
    for (int i = 0; i < CA; ++i) { const int c = tid + i * CT, r = c >> 3, c8 = c & 7;
      if (MODE == 0) ra[i] = *(const u32x4*)(p.xn + (size_t)(m0 + r) * 512 + k0 + c8 * 8);
      else ra[i] = __builtin_amdgcn_raw_buffer_load_b128(rox, ((m0 + r) * BHD + kz * K + k0 + c8 * 8) * 2, 0, AUX_SC1); }
#pragma unroll
    for (int i = 0; i < CB; ++i) { const int c = tid + i * CT, r = c >> 3, c8 = c & 7;
      if (c < BN * 8) rb[i] = *(const u32x4*)(Bt + (size_t)(n0 + r) * ldb + k0 + c8 * 8); }
  };
  gload(0);
  for (int k0 = 0; k0 < K; k0 += 64) {
    __syncthreads();
#pragma unroll
    for (int i = 0; i < CA; ++i) { const int c = tid + i * CT, r = c >> 3, c8 = c & 7; *(u32x4*)(As + r * LS + c8 * 8) = ra[i]; }
#pragma unroll
    for (int i = 0; i < CB; ++i) { const int c = tid + i * CT, r = c >> 3, c8 = c & 7; if (c < BN * 8) *(u32x4*)(Bs + r * LS + c8 * 8) = rb[i]; }
    __syncthreads();
    if (k0 + 64 < K) gload(k0 + 64);
#pragma unroll
    for (int kk = 0; kk < 2; ++kk) {
      bf16x8 af[MT], bfr[NT];
#pragma unroll
      for (int i = 0; i < MT; ++i) af[i] = *(const bf16x8*)(As + (wm * WM + i * 16 + fr) * LS + kk * 32 + fq * 8);
#pragma unroll
      for (int j = 0; j < NT; ++j) bfr[j] = *(const bf16x8*)(Bs + (wn * 32 + j * 16 + fr) * LS + kk * 32 + fq * 8);
#pragma unroll
      for (int i = 0; i < MT; ++i)
#pragma unroll
        for (int j = 0; j < NT; ++j) acc[i][j] = __builtin_amdgcn_mfma_f32_16x16x32_bf16(af[i], bfr[j], acc[i][j], 0, 0, 0);
    }
  }
  __syncthreads();
  if (MODE == 0) {
    if (n0 >= 2 * BHD) {
      const __amdgpu_buffer_rsrc_t rv = __builtin_amdgcn_make_buffer_rsrc((void*)p.vtx, 0, S_ * BHD * 2, 0x00020000);
#pragma unroll
      for (int i = 0; i < MT; ++i)
#pragma unroll
        for (int j = 0; j < NT; ++j) {
          const int n = n0 + wn * 32 + j * 16 + fr, m = m0 + wm * WM + i * 16 + fq * 4;
          u32x2 pk; pk[0] = pack2(acc[i][j][0], acc[i][j][1]); pk[1] = pack2(acc[i][j][2], acc[i][j][3]);
          __builtin_amdgcn_raw_buffer_store_b64(pk, rv, ((((m >> 5) * 32 + ((n - 2 * BHD) >> 7)) * 128 + (n & 127)) * 32 + (m & 31)) * 2, 0, AUX_SC1);
        }
    } else {
      constexpr int CS = BN + 8;
      u16* Cs = (u16*)smem;
#pragma unroll
      for (int i = 0; i < MT; ++i)
#pragma unroll
        for (int j = 0; j < NT; ++j)
#pragma unroll
          for (int r = 0; r < 4; ++r) Cs[(wm * WM + i * 16 + fq * 4 + r) * CS + wn * 32 + j * 16 + fr] = f2bf(acc[i][j][r]);
      __syncthreads();
      const __amdgpu_buffer_rsrc_t rq = __builtin_amdgcn_make_buffer_rsrc((void*)p.qkvx, 0, S_ * BN3 * 2, 0x00020000);
      for (int c = tid; c < BM * BN / 8; c += CT) { const int r = c / (BN / 8), c8 = c % (BN / 8);
        __builtin_amdgcn_raw_buffer_store_b128(*(const u32x4*)(Cs + r * CS + c8 * 8), rq, ((m0 + r) * BN3 + n0 + c8 * 8) * 2, 0, AUX_SC1); }
    }
  } else {
    constexpr int FS = BN + 4;
    float* Fs = (float*)smem;
#pragma unroll
    for (int i = 0; i < MT; ++i)
#pragma unroll
      for (int j = 0; j < NT; ++j)
#pragma unroll
        for (int r = 0; r < 4; ++r) Fs[(wm * WM + i * 16 + fq * 4 + r) * FS + wn * 32 + j * 16 + fr] = acc[i][j][r];
    __syncthreads();
    float* Pz = p.part + (size_t)kz * S_ * D_;
    for (int c = tid; c < BM * BN / 4; c += CT) { const int r = c / (BN / 4), c4 = c % (BN / 4);
      *(f32x4*)(Pz + (size_t)(m0 + r) * D_ + n0 + c4 * 4) = *(const f32x4*)(Fs + r * FS + c4 * 4); }
  }
}

DI void attn_task(const BArgs& p, int seg, int h, int lane) {
  const int fr = lane & 15, g = lane >> 4;
  const __amdgpu_buffer_rsrc_t rq = __builtin_amdgcn_make_buffer_rsrc((void*)p.qkvx, 0, S_ * BN3 * 2, 0x00020000);
  const __amdgpu_buffer_rsrc_t rk = __builtin_amdgcn_make_buffer_rsrc((void*)p.kmall, 0, NSEG * 8 * BHD * 2, 0x00020000);
  const __amdgpu_buffer_rsrc_t rvx = __builtin_amdgcn_make_buffer_rsrc((void*)p.vtx, 0, S_ * BHD * 2, 0x00020000);
  const __amdgpu_buffer_rsrc_t rvm = __builtin_amdgcn_make_buffer_rsrc((void*)p.vmT, 0, NSEG * 8 * BHD * 2, 0x00020000);
  const __amdgpu_buffer_rsrc_t rox = __builtin_amdgcn_make_buffer_rsrc((void*)p.ox, 0, S_ * BHD * 2, 0x00020000);
  bf16x8 qf[2][4];
#pragma unroll
  for (int ct = 0; ct < 2; ++ct)
#pragma unroll
    for (int ks = 0; ks < 4; ++ks)
      qf[ct][ks] = __builtin_bit_cast(bf16x8, __builtin_amdgcn_raw_buffer_load_b128(rq, ((seg * 32 + 16 * ct + fr) * BN3 + h * 128 + 8 * g + 32 * ks) * 2, 0, AUX_SC1));
  f32x4 sc[2][3];
#pragma unroll
  for (int t = 0; t < 3; ++t) {
    const int key = 16 * t + fr;
#pragma unroll
    for (int ct = 0; ct < 2; ++ct) sc[ct][t] = f32x4{0.f, 0.f, 0.f, 0.f};
#pragma unroll
    for (int ks = 0; ks < 4; ++ks) {
      u32x4 kv;
      if (key < 8) kv = __builtin_amdgcn_raw_buffer_load_b128(rk, ((seg * 8 + key) * BHD + h * 128 + 8 * g + 32 * ks) * 2, 0, AUX_SC1);
      else kv = __builtin_amdgcn_raw_buffer_load_b128(rq, ((seg * 32 + (key < 40 ? key - 8 : 0)) * BN3 + BHD + h * 128 + 8 * g + 32 * ks) * 2, 0, AUX_SC1);
      const bf16x8 kf = __builtin_bit_cast(bf16x8, kv);
#pragma unroll
      for (int ct = 0; ct < 2; ++ct) sc[ct][t] = __builtin_amdgcn_mfma_f32_16x16x32_bf16(kf, qf[ct][ks], sc[ct][t], 0, 0, 0);
    }
  }
  const float negbig = rbf(NEGBIG);
  bf16x8 pf[2][2];
#pragma unroll
  for (int ct = 0; ct < 2; ++ct) {
    const int row = BM_ + 16 * ct + fr;
    float pv[3][4]; float mx = -INFINITY;
#pragma unroll
    for (int t = 0; t < 3; ++t)
#pragma unroll
      for (int r = 0; r < 4; r += 2) {
        const unsigned c1 = cvtpk(sc[ct][t][r], sc[ct][t][r + 1]);
        const unsigned c2 = cvtpk(__uint_as_float(c1 << 16) * BSCALE, __uint_as_float(c1 & 0xffff0000u) * BSCALE);
        float v0 = __uint_as_float(c2 << 16), v1 = __uint_as_float(c2 & 0xffff0000u);
        const int key = 16 * t + 4 * g + r;
        if (key > row) v0 = negbig;
        if (key + 1 > row) v1 = negbig;
        if (t == 2 && g >= 2) { v0 = -INFINITY; v1 = -INFINITY; }
        pv[t][r] = v0; pv[t][r + 1] = v1; mx = max3f(mx, v0, v1);
      }
    mx = xmax16(mx); mx = xmax32(mx);
    float sum = 0.f;
#pragma unroll
    for (int t = 0; t < 3; ++t)
#pragma unroll
      for (int r = 0; r < 4; ++r) { pv[t][r] = __expf(pv[t][r] - mx); sum += pv[t][r]; }
    sum = xsum16(sum); sum = xsum32(sum);
    const float inv = 1.0f / sum;
    u32x4 p0, p1;
    p0[0] = cvtpk(pv[0][0] * inv, pv[0][1] * inv); p0[1] = cvtpk(pv[0][2] * inv, pv[0][3] * inv);
    p0[2] = cvtpk(pv[1][0] * inv, pv[1][1] * inv); p0[3] = cvtpk(pv[1][2] * inv, pv[1][3] * inv);
    p1[0] = cvtpk(pv[2][0] * inv, pv[2][1] * inv); p1[1] = cvtpk(pv[2][2] * inv, pv[2][3] * inv);
    p1[2] = 0u; p1[3] = 0u;
    pf[ct][0] = __builtin_bit_cast(bf16x8, p0); pf[ct][1] = __builtin_bit_cast(bf16x8, p1);
  }
  const int vb = (seg * 32 + h) * 128;
#pragma unroll
  for (int d = 0; d < 8; ++d) {
    const int dim = 16 * d + fr;
    const int vxo = (vb + dim) * 32 * 2;
    const u32x2 lo0 = g < 2 ? __builtin_amdgcn_raw_buffer_load_b64(rvm, ((vb + dim) * 8 + 4 * g) * 2, 0, AUX_SC1)
                            : __builtin_amdgcn_raw_buffer_load_b64(rvx, vxo + 8 * (g - 2), 0, AUX_SC1);
    const u32x2 hi0 = __builtin_amdgcn_raw_buffer_load_b64(rvx, vxo + (8 + 4 * g) * 2, 0, AUX_SC1);
    const u32x2 lo1 = __builtin_amdgcn_raw_buffer_load_b64(rvx, vxo + (24 + 4 * (g & 1)) * 2, 0, AUX_SC1);
    const u32x4 va0 = {lo0[0], lo0[1], hi0[0], hi0[1]}, va1 = {lo1[0], lo1[1], 0u, 0u};
#pragma unroll
    for (int ct = 0; ct < 2; ++ct) {
      f32x4 oa = {0.f, 0.f, 0.f, 0.f};
      oa = __builtin_amdgcn_mfma_f32_16x16x32_bf16(__builtin_bit_cast(bf16x8, va0), pf[ct][0], oa, 0, 0, 0);
      oa = __builtin_amdgcn_mfma_f32_16x16x32_bf16(__builtin_bit_cast(bf16x8, va1), pf[ct][1], oa, 0, 0, 0);
      u32x2 pk; pk[0] = cvtpk(oa[0], oa[1]); pk[1] = cvtpk(oa[2], oa[3]);
      __builtin_amdgcn_raw_buffer_store_b64(pk, rox, ((seg * 32 + 16 * ct + fr) * BHD + h * 128 + 16 * d + 4 * g) * 2, 0, AUX_SC1);
    }
  }
}

constexpr int NHELP = 192;
DI void helper_main(const BArgs& p, char* smem, int hid, int tid) {
  const int lane = tid & 63, w = __builtin_amdgcn_readfirstlane(tid >> 6);
  const __amdgpu_buffer_rsrc_t rc = __builtin_amdgcn_make_buffer_rsrc((void*)p.cnt, 0, CNT_LINES * 128, 0x00020000);
  const __amdgpu_buffer_rsrc_t rtm = __builtin_amdgcn_make_buffer_rsrc((void*)p.tmo, 0, 64, 0x00020000);
  for (int t = hid; t < 1536; t += NHELP) {
    int rb, cb;
    if (t < 1024) { rb = t >> 6; cb = 32 + (t & 63); } else { rb = (t - 1024) >> 5; cb = (t - 1024) & 31; }
    helper_gemm_tile<128, 0>(p, smem, rb * 128, cb * 128, 0, tid);
    VMDRAIN();
    __syncthreads();
    if (tid == 0) cnt_add(p.cnt + (t < 1024 ? CNT_KV + rb : CNT_Q + rb) * 32);
  }
  for (int t8 = hid; t8 < 256; t8 += NHELP) {
    const int seg = t8 >> 2, h = (t8 & 3) * 8 + w, rb = seg >> 2;
    if (w == 0) { cnt_wait(rc, CNT_KV + rb, 64, rtm); cnt_wait(rc, CNT_Q + rb, 32, rtm); cnt_wait(rc, CNT_SEG + seg, NCH, rtm); }
    __syncthreads();
    attn_task(p, seg, h, lane);
    VMDRAIN();
    if (lane == 0) cnt_add(p.cnt + (CNT_O + rb) * 32);
  }
  for (int it = hid; it < 512; it += NHELP) {
    const int rb = it >> 5, nb = (it >> 2) & 7, kz = it & 3;
    if (w == 0) cnt_wait(rc, CNT_O + rb, 128, rtm);
    __syncthreads();
    helper_gemm_tile<64, 1>(p, smem, rb * 128, nb * 64, kz, tid);
  }
}

__global__ __launch_bounds__(CT, 2) void blayer_kernel(BArgs p) {
  __shared__ __attribute__((aligned(16))) char smem[L_TOTAL];
  if (blockIdx.x >= NCH) { helper_main(p, smem, blockIdx.x - NCH, threadIdx.x); return; }
  const u16* __restrict__ Wqkv_t = p.Wqkv_t; const u16* __restrict__ Wo_t = p.Wo_t; const u16* __restrict__ mem0 = p.mem0;
  const u16* qkvx = p.qkvx; const u16* vtx = p.vtx; u16* kmall = p.kmall; u16* vmTall = p.vmT;
  unsigned* memx = p.memx; unsigned* inbox = p.inbox; unsigned* tmo = p.tmo;
  const int b = blockIdx.x, h = b >> 1, a = b & 1;
  const int tid = threadIdx.x, lane = tid & 63, w = __builtin_amdgcn_readfirstlane(tid >> 6), fr = lane & 15, g = lane >> 4;
  u16* VT = (u16*)(smem + L_VT); u16* KALL = (u16*)(smem + L_K); u16* QS = (u16*)(smem + L_Q); u16* OS = (u16*)(smem + L_O);
  const __amdgpu_buffer_rsrc_t rmx = __builtin_amdgcn_make_buffer_rsrc((void*)memx, 0, (NSEG + 1) * MEMX_SLOT_DW * 4, 0x00020000);
  const __amdgpu_buffer_rsrc_t rin = __builtin_amdgcn_make_buffer_rsrc((void*)inbox, 0, 2 * INBOX_DW * 4, 0x00020000);
  const __amdgpu_buffer_rsrc_t rtm = __builtin_amdgcn_make_buffer_rsrc((void*)tmo, 0, 64, 0x00020000);
  const __amdgpu_buffer_rsrc_t rqk = __builtin_amdgcn_make_buffer_rsrc((void*)qkvx, 0, S_ * BN3 * 2, 0x00020000);
  const __amdgpu_buffer_rsrc_t rvx = __builtin_amdgcn_make_buffer_rsrc((void*)vtx, 0, S_ * BHD * 2, 0x00020000);
  const __amdgpu_buffer_rsrc_t rkm = __builtin_amdgcn_make_buffer_rsrc((void*)kmall, 0, NSEG * 8 * BHD * 2, 0x00020000);
  const __amdgpu_buffer_rsrc_t rvm = __builtin_amdgcn_make_buffer_rsrc((void*)vmTall, 0, NSEG * 8 * BHD * 2, 0x00020000);
  const __amdgpu_buffer_rsrc_t rcn = __builtin_amdgcn_make_buffer_rsrc((void*)p.cnt, 0, CNT_LINES * 128, 0x00020000);

  bf16x8 wf[2][16];
#pragma unroll
  for (int t = 0; t < 2; ++t) {
    const u16* src = Wqkv_t + (size_t)(t * BHD + h * 128 + 16 * w + fr) * 512 + g * 8;
#pragma unroll
    for (int ks = 0; ks < 16; ++ks) wf[t][ks] = *(const bf16x8*)(src + ks * 32);
  }
  {
#pragma unroll
    for (int i = 0; i < 8; ++i) { const int f = w * 8 + i, j = f >> 4, ks = f & 15;
      *(bf16x8*)(smem + L_WV + (f * 64 + lane) * 16) = *(const bf16x8*)(Wqkv_t + (size_t)(2 * BHD + h * 128 + 64 * a + 16 * j + fr) * 512 + ks * 32 + g * 8); }
#pragma unroll
    for (int i = 0; i < 8; ++i) { const int f = w * 8 + i, nt = f >> 1, ks2 = f & 1;
      *(bf16x8*)(smem + L_WO + (f * 64 + lane) * 16) = *(const bf16x8*)(Wo_t + (size_t)(16 * nt + fr) * BHD + h * 128 + 64 * a + ks2 * 32 + g * 8); }
  }
  for (int c = tid; c < L_VT_BYTES / 16; c += CT) *(u32x4*)(smem + L_VT + c * 16) = u32x4{0u, 0u, 0u, 0u};
  LBAR();

  u32x4 kst, vst;
  const int ktok = tid >> 4, kd8 = tid & 15;
  const int vdim = tid >> 2, vc = tid & 3;
  const int k_voff = (ktok * BN3 + BHD + h * 128 + kd8 * 8) * 2;
  const int v_voff = ((h * 128 + 64 * a + vdim) * 32 + vc * 8) * 2;
  auto kv_load = [&](int s) {
    if ((s & 3) == 0) cnt_wait(rcn, CNT_KV + (s >> 2), 64, rtm);
    kst = __builtin_amdgcn_raw_buffer_load_b128(rqk, k_voff, s * 32 * BN3 * 2, AUX_SC1);
    if (tid < 256) vst = __builtin_amdgcn_raw_buffer_load_b128(rvx, v_voff, s * 32 * 128 * 32 * 2, AUX_SC1);
  };
  auto kv_store = [&]() {
    *(u32x4*)(KALL + (8 + ktok) * KS_LD + kd8 * 8) = kst;
    if (tid < 256) *(u32x4*)(VT + vdim * VT_LDB + 8 + vc * 8) = vst;
  };
  const int km_voff = (fr * BHD + h * 128 + 16 * w + 4 * g) * 2;
  const int vm_voff = ((h * 128 + 64 * a + 16 * w + fr) * 8 + 4 * g) * 2;
  const int ib_voff = (((g >> 1) * 64 + b) * 64 + fr * 8 + 4 * (g & 1)) * 4;
  const int ip_voff = (b * 64 * 64 + tid * 4) * 4;
  const int mp_voff = tid * 16;
  kv_load(0);
  kv_store();
  { const int r = tid >> 6, k = (tid & 63) * 8;
    *(u32x4*)(smem + L_MX + (k >> 3) * 128 + r * 16) = *(const u32x4*)(mem0 + r * 512 + k); }

  const char* mxl = smem + L_MX + g * 128 + (fr & 7) * 16;
  for (int s = 0; s < NSEG; ++s) {
    LBAR();
    if (s + 1 < NSEG) kv_load(s + 1);
    f32x4 acc[2];
#pragma unroll
    for (int t = 0; t < 2; ++t) acc[t] = f32x4{0.f, 0.f, 0.f, 0.f};
    {
      bf16x8 mq[4];
#pragma unroll
      for (int i = 0; i < 4; ++i) mq[i] = *(const bf16x8*)(mxl + i * 512);
#pragma unroll
      for (int ks = 0; ks < 16; ++ks) {
        const bf16x8 mf = mq[ks & 3];
        if (ks + 4 < 16) mq[ks & 3] = *(const bf16x8*)(mxl + (ks + 4) * 512);
        acc[0] = __builtin_amdgcn_mfma_f32_16x16x32_bf16(wf[0][ks], mf, acc[0], 0, 0, 0);
        acc[1] = __builtin_amdgcn_mfma_f32_16x16x32_bf16(wf[1][ks], mf, acc[1], 0, 0, 0);
      }
    }
    STAMP(0);
    {
      const int n0 = 16 * w + 4 * g;
      u32x2 pq, pk2;
      pq[0] = pack2(acc[0][0], acc[0][1]); pq[1] = pack2(acc[0][2], acc[0][3]);
      pk2[0] = pack2(acc[1][0], acc[1][1]); pk2[1] = pack2(acc[1][2], acc[1][3]);
      if (fr < 8) { *(u32x2*)(QS + fr * KS_LD + n0) = pq; *(u32x2*)(KALL + fr * KS_LD + n0) = pk2;
                    if (a == 0) __builtin_amdgcn_raw_buffer_store_b64(pk2, rkm, km_voff, s * 8 * BHD * 2, AUX_SC1); }
    }
    LBAR();
    STAMP(1);
    if (w >= 4) __builtin_amdgcn_s_setprio(3);
    bf16x8 pf[2];
    if (w < 4) {
      const char* wvb = smem + L_WV + (w * 16 * 64 + lane) * 16;
      f32x4 av = {0.f, 0.f, 0.f, 0.f};
      bf16x8 mfa = *(const bf16x8*)(mxl), mfb = *(const bf16x8*)(mxl + 512);
      bf16x8 wva = *(const bf16x8*)(wvb), wvb2 = *(const bf16x8*)(wvb + 1024);
#pragma unroll
      for (int ks = 0; ks < 16; ++ks) {
        const bf16x8 mf = (ks & 1) ? mfb : mfa, wv = (ks & 1) ? wvb2 : wva;
        if (ks + 2 < 16) {
          if (ks & 1) { mfb = *(const bf16x8*)(mxl + (ks + 2) * 512); wvb2 = *(const bf16x8*)(wvb + (ks + 2) * 1024); }
          else { mfa = *(const bf16x8*)(mxl + (ks + 2) * 512); wva = *(const bf16x8*)(wvb + (ks + 2) * 1024); }
        }
        av = __builtin_amdgcn_mfma_f32_16x16x32_bf16(mf, wv, av, 0, 0, 0);
      }
      u32x2 pv2; pv2[0] = pack2(av[0], av[1]); pv2[1] = pack2(av[2], av[3]);
      if (g < 2) { *(u32x2*)(VT + (16 * w + fr) * VT_LDB + 4 * g) = pv2; __builtin_amdgcn_raw_buffer_store_b64(pv2, rvm, vm_voff, s * 32 * 128 * 8 * 2, AUX_SC1); }
    } else {
      bf16x8 qf[4];
#pragma unroll
      for (int ks = 0; ks < 4; ++ks) qf[ks] = *(const bf16x8*)(QS + (fr & 7) * KS_LD + 32 * ks + 8 * g);
      f32x4 sc[3];
      {
        const u16* kb = KALL + fr * KS_LD + 8 * g;
        bf16x8 kf[3][4];
#pragma unroll
        for (int t = 0; t < 3; ++t)
#pragma unroll
          for (int ks = 0; ks < 4; ++ks) kf[t][ks] = *(const bf16x8*)(kb + 16 * t * KS_LD + 32 * ks);
#pragma unroll
        for (int t = 0; t < 3; ++t) sc[t] = f32x4{0.f, 0.f, 0.f, 0.f};
#pragma unroll
        for (int ks = 0; ks < 4; ++ks)
#pragma unroll
          for (int t = 0; t < 3; ++t) sc[t] = __builtin_amdgcn_mfma_f32_16x16x32_bf16(kf[t][ks], qf[ks], sc[t], 0, 0, 0);
      }
      float pv[3][4]; float mx = -INFINITY;
#pragma unroll
      for (int t = 0; t < 3; ++t)
#pragma unroll
        for (int r = 0; r < 4; r += 2) {
          const unsigned c1 = pack2(sc[t][r], sc[t][r + 1]);
          const unsigned c2 = pack2(__uint_as_float(c1 << 16) * BSCALE, __uint_as_float(c1 & 0xffff0000u) * BSCALE);
          float v0 = __uint_as_float(c2 << 16), v1 = __uint_as_float(c2 & 0xffff0000u);
          if (t == 2 && g >= 2) { v0 = -INFINITY; v1 = -INFINITY; }
          pv[t][r] = v0; pv[t][r + 1] = v1; mx = max3f(mx, v0, v1);
        }
      mx = xmax16(mx); mx = xmax32(mx);
      float sum = 0.f;
#pragma unroll
      for (int t = 0; t < 3; ++t)
#pragma unroll
        for (int r = 0; r < 4; ++r) { pv[t][r] = __expf(pv[t][r] - mx); sum += pv[t][r]; }
      sum = xsum16(sum); sum = xsum32(sum);
      const float inv = 1.0f / sum;
      u32x4 p0, p1;
      p0[0] = pack2(pv[0][0] * inv, pv[0][1] * inv); p0[1] = pack2(pv[0][2] * inv, pv[0][3] * inv);
      p0[2] = pack2(pv[1][0] * inv, pv[1][1] * inv); p0[3] = pack2(pv[1][2] * inv, pv[1][3] * inv);
      p1[0] = pack2(pv[2][0] * inv, pv[2][1] * inv); p1[1] = pack2(pv[2][2] * inv, pv[2][3] * inv);
      p1[2] = 0u; p1[3] = 0u;
      pf[0] = __builtin_bit_cast(bf16x8, p0); pf[1] = __builtin_bit_cast(bf16x8, p1);
    }
    LBAR();
    if (w >= 4) {
      const u16* vrow = VT + (16 * (w & 3) + fr) * VT_LDB + 4 * g;
      const u32x2 v00 = *(const u32x2*)(vrow), v01 = *(const u32x2*)(vrow + 16), v10 = *(const u32x2*)(vrow + 32), v11 = *(const u32x2*)(vrow + 48);
      const u32x4 va0 = {v00[0], v00[1], v01[0], v01[1]}, va1 = {v10[0], v10[1], v11[0], v11[1]};
      f32x4 oa = {0.f, 0.f, 0.f, 0.f};
      oa = __builtin_amdgcn_mfma_f32_16x16x32_bf16(__builtin_bit_cast(bf16x8, va0), pf[0], oa, 0, 0, 0);
      oa = __builtin_amdgcn_mfma_f32_16x16x32_bf16(__builtin_bit_cast(bf16x8, va1), pf[1], oa, 0, 0, 0);
      u32x2 pk; pk[0] = pack2(oa[0], oa[1]); pk[1] = pack2(oa[2], oa[3]);
      if (fr < 8) *(u32x2*)(OS + fr * OS_LD + 16 * (w & 3) + 4 * g) = pk;
    }
    __builtin_amdgcn_s_setprio(0);
    bf16x8 wo[8];
    {
      const char* wob = smem + L_WO + (w * 8 * 64 + lane) * 16;
#pragma unroll
      for (int i = 0; i < 8; ++i) wo[i] = *(const bf16x8*)(wob + i * 1024);
    }
    LBAR();
    STAMP(2);
    if (s + 1 == NSEG) { VMDRAIN(); LBAR(); if (tid == 0) cnt_add(p.cnt + (CNT_SEG + s) * 32); break; }
    const int slot = s & 1;
    {
      bf16x8 of[2];
#pragma unroll
      for (int ks = 0; ks < 2; ++ks) of[ks] = *(const bf16x8*)(OS + (fr & 7) * OS_LD + 32 * ks + 8 * g);
#pragma unroll
      for (int i = 0; i < 4; ++i) {
        f32x4 acc2 = {0.f, 0.f, 0.f, 0.f};
        acc2 = __builtin_amdgcn_mfma_f32_16x16x32_bf16(wo[2 * i], of[0], acc2, 0, 0, 0);
        acc2 = __builtin_amdgcn_mfma_f32_16x16x32_bf16(wo[2 * i + 1], of[1], acc2, 0, 0, 0);
        u32x4 st;
#pragma unroll
        for (int r = 0; r < 4; ++r) { const unsigned ub = __float_as_uint(acc2[r]); st[r] = ub == SENT ? 0x7FC00000u : ub; }
        if (fr < 8) __builtin_amdgcn_raw_buffer_store_b128(st, rin, ib_voff, (slot * INBOX_DW + (4 * w + i) * 2 * 64 * 64) * 4, AUX_SC1);
      }
    }
    STAMP(3);
    kv_store();
    STAMP(4);
#pragma unroll
    for (int i = 0; i < CH_DELAY1; ++i) __builtin_amdgcn_s_sleep(4);
    u32x4 pib[2];
    {
      unsigned spins = 0;
      for (;;) {
        bool ok = true;
#pragma unroll
        for (int i = 0; i < 2; ++i) { pib[i] = __builtin_amdgcn_raw_buffer_load_b128(rin, ip_voff, (slot * INBOX_DW + CT * i * 4) * 4, AUX_SC1); ok &= valid4(pib[i]); }
        if (__all(ok)) break;
        if ((++spins & 63u) == 0u) {
          const unsigned t0 = __builtin_amdgcn_readfirstlane(__builtin_amdgcn_raw_buffer_load_b32(rtm, 0, 0, AUX_SC1));
          if (spins > SPIN_LIMIT || t0 == 1u) { if (lane == 0) __builtin_amdgcn_raw_buffer_store_b32(1u, rtm, 0, 0, AUX_SC1); break; }
        }
        __builtin_amdgcn_s_sleep(2);
      }
    }
    STAMP(5);
    {
      f32x4 ps;
#pragma unroll
      for (int r = 0; r < 4; ++r) ps[r] = xsum32(xsum16(__uint_as_float(pib[0][r]) + __uint_as_float(pib[1][r])));
      if (lane < 16) *(f32x4*)(smem + L_RS + (w * 16 + lane) * 16) = ps;
    }
    LBAR();
    if (tid == 64) cnt_add(p.cnt + (CNT_SEG + s) * 32);
    if (tid < 16) {
      f32x4 t4 = *(const f32x4*)(smem + L_RS + tid * 16);
#pragma unroll
      for (int j = 1; j < 8; ++j) { const f32x4 o4 = *(const f32x4*)(smem + L_RS + (j * 16 + tid) * 16); t4 = t4 + o4; }
      u32x2 pk; pk[0] = canon(pack2(t4[0], t4[1])); pk[1] = canon(pack2(t4[2], t4[3]));
      __builtin_amdgcn_raw_buffer_store_b64(pk, rmx, (b * 32 + tid * 2) * 4, (s + 1) * MEMX_SLOT_DW * 4, AUX_SC1);
    }
    {
      const u32x4 sent = {SENT, SENT, SENT, SENT};
#pragma unroll
      for (int i = 0; i < 2; ++i) __builtin_amdgcn_raw_buffer_store_b128(sent, rin, ip_voff, (slot * INBOX_DW + CT * i * 4) * 4, AUX_SC1);
    }
    STAMP(6);
#pragma unroll
    for (int i = 0; i < CH_DELAY2; ++i) __builtin_amdgcn_s_sleep(4);
    {
      u32x4 m;
      unsigned spins = 0;
      for (;;) {
        m = __builtin_amdgcn_raw_buffer_load_b128(rmx, mp_voff, (s + 1) * MEMX_SLOT_DW * 4, AUX_SC1);
        if (__all(valid4(m))) break;
        if ((++spins & 63u) == 0u) {
          const unsigned t0 = __builtin_amdgcn_readfirstlane(__builtin_amdgcn_raw_buffer_load_b32(rtm, 0, 0, AUX_SC1));
          if (spins > SPIN_LIMIT || t0 == 1u) { if (lane == 0) __builtin_amdgcn_raw_buffer_store_b32(1u, rtm, 0, 0, AUX_SC1); break; }
        }
        __builtin_amdgcn_s_sleep(2);
      }
      *(u32x4*)(smem + L_MX + tid * 16) = m;
    }
    STAMP(7);
  }
}

extern "C" void kernel_launch(void* const* d_in, const int* in_sizes, int n_in,
                              void* d_out, int out_size, void* d_ws, size_t ws_size,
                              hipStream_t stream) {
  const u16* A_x = (const u16*)d_in[0];
  const u16* A_wq = (const u16*)d_in[1];
  const u16* A_wk = (const u16*)d_in[2];
  const u16* A_wv = (const u16*)d_in[3];
  const u16* A_wo = (const u16*)d_in[4];
  const u16* B_mem0 = (const u16*)d_in[6];
  const u16* B_Wq = (const u16*)d_in[7];
  const u16* B_Wk = (const u16*)d_in[8];
  const u16* B_Wv = (const u16*)d_in[9];
  const u16* B_Wo = (const u16*)d_in[10];
  u16* out = (u16*)d_out;

  char* ws = (char*)d_ws; size_t off = 0;
  auto alloc = [&](size_t bytes) { char* p = ws + off; off += (bytes + 255) & ~(size_t)255; return (u16*)p; };
  u16* xn = alloc((size_t)S_ * D_ * 2);
  u16* x1 = alloc((size_t)S_ * D_ * 2);
  u16* x2 = alloc((size_t)S_ * D_ * 2);
  u16* x3 = alloc((size_t)S_ * D_ * 2);
  u16* aqkv = alloc((size_t)S_ * AQ_LD * 2);
  u16* ao = alloc((size_t)S_ * D_ * 2);
  u16* Awqkv_t = alloc((size_t)AQ_LD * D_ * 2);
  u16* Awo_t = alloc((size_t)D_ * D_ * 2);
  u16* BWqkv_t = alloc((size_t)BN3 * D_ * 2);
  u16* BWo_t = alloc((size_t)D_ * BHD * 2);
  u16* qkvx = alloc((size_t)S_ * BN3 * 2);
  u16* ox = alloc((size_t)S_ * BHD * 2);
  u16* vtx = alloc((size_t)S_ * BHD * 2);
  u16* akvx = alloc((size_t)S_ * 32 * 2);
  u16* kmall = alloc((size_t)NSEG * 8 * BHD * 2);
  u16* vmall = alloc((size_t)NSEG * 8 * BHD * 2);
  char* exch0 = ws + off;
  unsigned* tmo = (unsigned*)alloc(256 + 64 * 8 * 8 * 8);
  unsigned* memx = (unsigned*)alloc((size_t)2 * (NSEG + 1) * MEMX_SLOT_DW * 4);
  unsigned* inbox = (unsigned*)alloc((size_t)2 * INBOX_DW * 4);
  unsigned* cnts = (unsigned*)alloc((size_t)2 * CNT_LINES * 128);
  hipMemsetAsync(exch0, 0xFF, (size_t)((ws + off) - exch0), stream);
  int blayer = 0;

  {
    TrJobs jb;
    const u16* srcs[8] = {A_wq, A_wk, A_wv, A_wo, B_Wq, B_Wk, B_Wv, B_Wo};
    u16* dsts[8] = {Awqkv_t, Awqkv_t + 512 * 512, Awqkv_t + 528 * 512, Awo_t, BWqkv_t, BWqkv_t + (size_t)4096 * 512, BWqkv_t + (size_t)8192 * 512, BWo_t};
    const int Ks[8] = {512, 512, 512, 512, 512, 512, 512, 4096}, Ns[8] = {512, 16, 16, 512, 4096, 4096, 4096, 512};
    int t0 = 0;
    for (int i = 0; i < 8; ++i) { jb.src[i] = srcs[i]; jb.dst[i] = dsts[i]; jb.K[i] = Ks[i]; jb.N[i] = Ns[i]; jb.tile0[i] = t0; t0 += (Ks[i] / 64) * ((Ns[i] + 63) / 64); }
    jb.tile0[8] = t0; jb.pad_ = 0;
    transpose_all_kernel<<<t0, 256, 0, stream>>>(jb);
  }

  float* part = (float*)alloc((size_t)4 * S_ * D_ * 4);
  auto layerA = [&](const u16* xin, u16* xout, bool ln_next) {
    gemm_nt<64, 64, 3><<<dim3(9, 32), 256, 0, stream>>>(xn, 512, Awqkv_t, 512, aqkv, AQ_LD, AQ_LD, 512, nullptr, akvx);
    attnA_kernel<<<256, 512, 0, stream>>>(aqkv, akvx, ao);
    gemm_nt<64, 64, 4><<<dim3(8, 32, 1), 256, 0, stream>>>(ao, 512, Awo_t, 512, nullptr, 512, 512, 512, nullptr, (u16*)part);
    finish_ln_kernel<1><<<512, 256, 0, stream>>>(part, xin, xout, ln_next ? xn : nullptr);
  };
  auto layerB = [&](const u16* xin, u16* xout, bool ln_next) {
    BArgs ba;
    ba.xn = xn; ba.Wqkv_t = BWqkv_t; ba.Wo_t = BWo_t; ba.mem0 = B_mem0;
    ba.qkvx = qkvx; ba.vtx = vtx; ba.kmall = kmall; ba.vmT = vmall; ba.ox = ox; ba.part = part;
    ba.memx = memx + (size_t)blayer * (NSEG + 1) * MEMX_SLOT_DW; ba.inbox = inbox; ba.tmo = tmo; ba.cnt = cnts + blayer * CNT_LINES * 32;
    blayer_kernel<<<NCH + NHELP, CT, 0, stream>>>(ba);
    ++blayer;
    finish_ln_kernel<4><<<512, 256, 0, stream>>>(part, xin, xout, ln_next ? xn : nullptr);
  };
  ln_kernel<<<512, 256, 0, stream>>>(A_x, xn);
  layerA(A_x, x1, true);
  layerB(x1, x2, true);
  layerA(x2, x3, true);
  layerB(x3, out, false);
}
```

```cpp
#include <hip/hip_runtime.h>
#include <hip/hip_bf16.h>
#include <stdint.h>

typedef unsigned short u16;
using bf16x8 = __attribute__((ext_vector_type(8))) short;
using f32x4  = __attribute__((ext_vector_type(4))) float;
using f32x16 = __attribute__((ext_vector_type(16))) float;
using u32x4  = __attribute__((ext_vector_type(4))) unsigned;
using u32x2  = __attribute__((ext_vector_type(2))) unsigned;
#define DI __device__ __forceinline__

DI float bf2f(u16 x) { return __uint_as_float(((unsigned)x) << 16); }
DI u16 f2bf(float f) { __bf16 b = (__bf16)f; return __builtin_bit_cast(u16, b); }
DI float rbf(float f) { return bf2f(f2bf(f)); }
typedef __attribute__((ext_vector_type(2))) __bf16 bf16x2_t;
typedef __attribute__((ext_vector_type(2))) float f32x2_t;
DI unsigned cvtpk(float lo, float hi) { const f32x2_t v = {lo, hi}; return __builtin_bit_cast(unsigned, __builtin_convertvector(v, bf16x2_t)); }
DI unsigned pack2(float lo, float hi) { return cvtpk(lo, hi); }
DI float max3f(float a, float b, float c) { float r; asm("v_max3_f32 %0, %1, %2, %3" : "=v"(r) : "v"(a), "v"(b), "v"(c)); return r; }
DI float max3f_pad(float a, float b, float c) { float r; asm("v_max3_f32 %0, %1, %2, %3\n\ts_nop 1" : "=v"(r) : "v"(a), "v"(b), "v"(c)); return r; }
DI float xmax16(float x) { x = max3f_pad(x, x, x); auto r = __builtin_amdgcn_permlane16_swap(__float_as_uint(x), __float_as_uint(x), false, false); return max3f(__uint_as_float(r[0]), __uint_as_float(r[1]), __uint_as_float(r[1])); }
DI float xmax32(float x) { x = max3f_pad(x, x, x); auto r = __builtin_amdgcn_permlane32_swap(__float_as_uint(x), __float_as_uint(x), false, false); return max3f(__uint_as_float(r[0]), __uint_as_float(r[1]), __uint_as_float(r[1])); }
DI float xsum16(float x) { auto r = __builtin_amdgcn_permlane16_swap(__float_as_uint(x), __float_as_uint(x), false, false); return __uint_as_float(r[0]) + __uint_as_float(r[1]); }
DI float xsum32(float x) { auto r = __builtin_amdgcn_permlane32_swap(__float_as_uint(x), __float_as_uint(x), false, false); return __uint_as_float(r[0]) + __uint_as_float(r[1]); }

constexpr int S_ = 2048, D_ = 512;
constexpr int AH = 32, ADH = 16;
constexpr int BH = 32, BDH = 128, BM_ = 8, SEG = 32, NSEG = 64;
constexpr int BN3 = 3 * BH * BDH;
constexpr int BHD = BH * BDH;

DI void ln_row(const u16* __restrict__ x, u16* __restrict__ y, int row, int lane) {
  const u32x4 v = *(const u32x4*)(x + (size_t)row * D_ + lane * 8);
  float f[8];
#pragma unroll
  for (int i = 0; i < 4; ++i) { f[2 * i] = __uint_as_float(v[i] << 16); f[2 * i + 1] = __uint_as_float(v[i] & 0xffff0000u); }
  float s = 0.f;
#pragma unroll
  for (int i = 0; i < 8; ++i) s += f[i];
#pragma unroll
  for (int o = 32; o >= 1; o >>= 1) s += __shfl_xor(s, o);
  const float mean = s * (1.0f / D_);
  float q = 0.f;
#pragma unroll
  for (int i = 0; i < 8; ++i) { const float d = f[i] - mean; q += d * d; }
#pragma unroll
  for (int o = 32; o >= 1; o >>= 1) q += __shfl_xor(q, o);
  const float var = q * (1.0f / D_);
  const float mb = rbf(mean), vb = rbf(var);
  const float den = rbf(sqrtf(rbf(vb + 1e-5f)));
  u32x4 o;
#pragma unroll
  for (int i = 0; i < 4; ++i) o[i] = pack2(rbf(f[2 * i] - mb) / den, rbf(f[2 * i + 1] - mb) / den);
  *(u32x4*)(y + (size_t)row * D_ + lane * 8) = o;
}

template <int NP>
__global__ __launch_bounds__(256) void finish_ln_kernel(const float* __restrict__ P, const u16* __restrict__ resid, u16* __restrict__ xo, u16* __restrict__ y) {
  const int row = blockIdx.x * 4 + (threadIdx.x >> 6), lane = threadIdx.x & 63;
  float f[8];
  {
    f32x4 a0 = *(const f32x4*)(P + (size_t)row * D_ + lane * 8), a1 = *(const f32x4*)(P + (size_t)row * D_ + lane * 8 + 4);
#pragma unroll
    for (int z = 1; z < NP; ++z) {
      const f32x4 b0 = *(const f32x4*)(P + (size_t)z * S_ * D_ + (size_t)row * D_ + lane * 8), b1 = *(const f32x4*)(P + (size_t)z * S_ * D_ + (size_t)row * D_ + lane * 8 + 4);
      a0 = a0 + b0; a1 = a1 + b1;
    }
    const u32x4 rv = *(const u32x4*)(resid + (size_t)row * D_ + lane * 8);
#pragma unroll
    for (int i = 0; i < 4; ++i) {
      const unsigned t = i < 2 ? cvtpk(a0[2 * i], a0[2 * i + 1]) : cvtpk(a1[2 * i - 4], a1[2 * i - 3]);
      const unsigned xs = cvtpk(__uint_as_float(t << 16) + __uint_as_float(rv[i] << 16), __uint_as_float(t & 0xffff0000u) + __uint_as_float(rv[i] & 0xffff0000u));
      f[2 * i] = __uint_as_float(xs << 16); f[2 * i + 1] = __uint_as_float(xs & 0xffff0000u);
    }
    u32x4 o;
#pragma unroll
    for (int i = 0; i < 4; ++i) o[i] = cvtpk(f[2 * i], f[2 * i + 1]);
    *(u32x4*)(xo + (size_t)row * D_ + lane * 8) = o;
  }
  if (y == nullptr) return;
  float s = 0.f;
#pragma unroll
  for (int i = 0; i < 8; ++i) s += f[i];
#pragma unroll
  for (int o = 32; o >= 1; o >>= 1) s += __shfl_xor(s, o);
  const float mean = s * (1.0f / D_);
  float q = 0.f;
#pragma unroll
  for (int i = 0; i < 8; ++i) { const float d = f[i] - mean; q += d * d; }
#pragma unroll
  for (int o = 32; o >= 1; o >>= 1) q += __shfl_xor(q, o);
  const float var = q * (1.0f / D_);
  const float mb = rbf(mean), vb = rbf(var);
  const float den = rbf(sqrtf(rbf(vb + 1e-5f)));
  u32x4 o;
#pragma unroll
  for (int i = 0; i < 4; ++i) o[i] = pack2(rbf(f[2 * i] - mb) / den, rbf(f[2 * i + 1] - mb) / den);
  *(u32x4*)(y + (size_t)row * D_ + lane * 8) = o;
}

struct TrJobs { const u16* src[8]; u16* dst[8]; unsigned* fill; const u16* lnx; u16* lny; int K[8]; int N[8]; int tile0[9]; int fill_u4; };
DI void ln_row(const u16* __restrict__ x, u16* __restrict__ y, int row, int lane);
__global__ __launch_bounds__(256) void prologue_kernel(TrJobs jobs) {
  __shared__ __attribute__((aligned(16))) u16 t[64][68];
  const int nt = jobs.tile0[8];
  if ((int)blockIdx.x >= nt) {
    const int e = blockIdx.x - nt;
    if (e < 64) { for (int i = e * 256 + threadIdx.x; i < jobs.fill_u4; i += 64 * 256) ((u32x4*)jobs.fill)[i] = u32x4{0xFFFFFFFFu, 0xFFFFFFFFu, 0xFFFFFFFFu, 0xFFFFFFFFu}; }
    else ln_row(jobs.lnx, jobs.lny, (e - 64) * 4 + (threadIdx.x >> 6), threadIdx.x & 63);
    return;
  }
  int j = 0;
#pragma unroll
  for (int i = 1; i < 8; ++i) j += (int)blockIdx.x >= jobs.tile0[i];
  const int K = jobs.K[j], N = jobs.N[j], tl = blockIdx.x - jobs.tile0[j];
  const int ntn = (N + 63) >> 6, n0 = (tl % ntn) * 64, k0 = (tl / ntn) * 64;
  const u16* __restrict__ in = jobs.src[j]; u16* __restrict__ out = jobs.dst[j];
  const int tx = threadIdx.x & 15, ty = threadIdx.x >> 4;
#pragma unroll
  for (int i = 0; i < 4; ++i) {
    const int r = ty + 16 * i, c = tx * 4;
    if (n0 + c < N) {
      const u32x2 v = *(const u32x2*)(in + (size_t)(k0 + r) * N + n0 + c);
      t[c][r] = (u16)(v[0] & 0xffff); t[c + 1][r] = (u16)(v[0] >> 16); t[c + 2][r] = (u16)(v[1] & 0xffff); t[c + 3][r] = (u16)(v[1] >> 16);
    }
  }
  __syncthreads();
#pragma unroll
  for (int i = 0; i < 4; ++i) {
    const int n = ty + 16 * i;
    if (n0 + n < N) *(u32x2*)(out + (size_t)(n0 + n) * K + k0 + tx * 4) = *(const u32x2*)(&t[n][tx * 4]);
  }
}

template <int BM, int BN, int EPI>
__global__ __launch_bounds__(256) void gemm_nt(const u16* __restrict__ A, int lda, const u16* __restrict__ Bt, int ldb,
                                               u16* __restrict__ C, int ldc, int N, int K, const u16* __restrict__ R, u16* __restrict__ X) {
  constexpr int BK = 64, LS = 72;
  constexpr int MT = BM / 32, NT = BN / 32;
  constexpr int CA = BM * 8 / 256, CB = BN * 8 / 256;
  constexpr int LDS_AB = (BM + BN) * LS * 2, LDS_C = EPI == 4 ? BM * (BN + 4) * 4 : BM * (BN + 8) * 2;
  __shared__ __attribute__((aligned(16))) char smem[LDS_AB > LDS_C ? LDS_AB : LDS_C];
  u16* As = (u16*)smem; u16* Bs = As + BM * LS;
  const int tid = threadIdx.x, lane = tid & 63, w = tid >> 6, wm = w >> 1, wn = w & 1;
  const int m0 = blockIdx.y * BM, n0 = blockIdx.x * BN;
  const int fr = lane & 15, fq = lane >> 4;
  if (EPI == 4) { A += (size_t)blockIdx.z * K; Bt += (size_t)blockIdx.z * K; }
  f32x4 acc[MT][NT];
#pragma unroll
  for (int i = 0; i < MT; ++i)
#pragma unroll
    for (int j = 0; j < NT; ++j) acc[i][j] = f32x4{0.f, 0.f, 0.f, 0.f};
  u32x4 ra[CA], rb[CB];
  auto gload = [&](int k0) {
#pragma unroll
    for (int i = 0; i < CA; ++i) { const int c = tid + i * 256, r = c >> 3, c8 = c & 7; ra[i] = *(const u32x4*)(A + (size_t)(m0 + r) * lda + k0 + c8 * 8); }
#pragma unroll
    for (int i = 0; i < CB; ++i) { const int c = tid + i * 256, r = c >> 3, c8 = c & 7; int n = n0 + r; n = n < N ? n : N - 1;
      rb[i] = *(const u32x4*)(Bt + (size_t)n * ldb + k0 + c8 * 8); }
  };
  gload(0);
  for (int k0 = 0; k0 < K; k0 += BK) {
    __syncthreads();
#pragma unroll
    for (int i = 0; i < CA; ++i) { const int c = tid + i * 256, r = c >> 3, c8 = c & 7; *(u32x4*)(As + r * LS + c8 * 8) = ra[i]; }
#pragma unroll
    for (int i = 0; i < CB; ++i) { const int c = tid + i * 256, r = c >> 3, c8 = c & 7; *(u32x4*)(Bs + r * LS + c8 * 8) = rb[i]; }
    __syncthreads();
    if (k0 + BK < K) gload(k0 + BK);
#pragma unroll
    for (int kk = 0; kk < 2; ++kk) {
      bf16x8 af[MT], bfr[NT];
#pragma unroll
      for (int i = 0; i < MT; ++i) af[i] = *(const bf16x8*)(As + (wm * (BM / 2) + i * 16 + fr) * LS + kk * 32 + fq * 8);
#pragma unroll
      for (int j = 0; j < NT; ++j) bfr[j] = *(const bf16x8*)(Bs + (wn * (BN / 2) + j * 16 + fr) * LS + kk * 32 + fq * 8);
#pragma unroll
      for (int i = 0; i < MT; ++i)
#pragma unroll
        for (int j = 0; j < NT; ++j) acc[i][j] = __builtin_amdgcn_mfma_f32_16x16x32_bf16(af[i], bfr[j], acc[i][j], 0, 0, 0);
    }
  }
  if ((EPI == 2 && n0 >= 2 * BHD) || (EPI == 3 && n0 + BN > 512)) {
#pragma unroll
    for (int i = 0; i < MT; ++i)
#pragma unroll
      for (int j = 0; j < NT; ++j) {
        const int n = n0 + wn * (BN / 2) + j * 16 + fr, m = m0 + wm * (BM / 2) + i * 16 + fq * 4;
        u32x2 pk; pk[0] = pack2(acc[i][j][0], acc[i][j][1]); pk[1] = pack2(acc[i][j][2], acc[i][j][3]);
        if (EPI == 2) {
          const int hh = (n - 2 * BHD) >> 7, dim = n & 127;
          *(u32x2*)(X + ((size_t)((m >> 5) * 32 + hh) * 128 + dim) * 32 + (m & 31)) = pk;
        } else if (n >= 512 && n < 528) {
#pragma unroll
          for (int r = 0; r < 4; ++r) X[(size_t)(m + r) * 16 + (n - 512)] = (u16)(r & 1 ? pk[r >> 1] >> 16 : pk[r >> 1] & 0xffff);
        } else if (n >= 528 && n < 544) {
          const int pos = (m & ~15) | ((fq & 1) * 8 + (fq >> 1) * 4);
          *(u32x2*)(X + 2048 * 16 + (size_t)(n - 528) * 2048 + pos) = pk;
        } else if (n < 512) {
#pragma unroll
          for (int r = 0; r < 4; ++r) C[(size_t)(m + r) * ldc + n] = (u16)(r & 1 ? pk[r >> 1] >> 16 : pk[r >> 1] & 0xffff);
        }
      }
    return;
  }
  if (EPI == 4) {
    __syncthreads();
    constexpr int FS = BN + 4;
    float* Fs = (float*)smem;
#pragma unroll
    for (int i = 0; i < MT; ++i)
#pragma unroll
      for (int j = 0; j < NT; ++j)
#pragma unroll
        for (int r = 0; r < 4; ++r) Fs[(wm * (BM / 2) + i * 16 + fq * 4 + r) * FS + wn * (BN / 2) + j * 16 + fr] = acc[i][j][r];
    __syncthreads();
    float* Pz = (float*)X + (size_t)blockIdx.z * S_ * ldc;
    for (int c = tid; c < BM * BN / 4; c += 256) { const int r = c / (BN / 4), c4 = c % (BN / 4);
      *(f32x4*)(Pz + (size_t)(m0 + r) * ldc + n0 + c4 * 4) = *(const f32x4*)(Fs + r * FS + c4 * 4); }
    return;
  }
  __syncthreads();
  constexpr int CS = BN + 8;
  u16* Cs = (u16*)smem;
#pragma unroll
  for (int i = 0; i < MT; ++i)
#pragma unroll
    for (int j = 0; j < NT; ++j)
#pragma unroll
      for (int r = 0; r < 4; ++r) Cs[(wm * (BM / 2) + i * 16 + fq * 4 + r) * CS + wn * (BN / 2) + j * 16 + fr] = f2bf(acc[i][j][r]);
  __syncthreads();
  for (int c = tid; c < BM * BN / 8; c += 256) {
    const int r = c / (BN / 8), c8 = c % (BN / 8);
    const int n = n0 + c8 * 8;
    if (n >= N) continue;
    u32x4 v = *(const u32x4*)(Cs + r * CS + c8 * 8);
    if (EPI == 1) {
      const u32x4 rr = *(const u32x4*)(R + (size_t)(m0 + r) * ldc + n);
#pragma unroll
      for (int i = 0; i < 4; ++i) {
        const float lo = bf2f((u16)(v[i] & 0xffff)) + bf2f((u16)(rr[i] & 0xffff));
        const float hi = bf2f((u16)(v[i] >> 16)) + bf2f((u16)(rr[i] >> 16));
        v[i] = pack2(lo, hi);
      }
    }
    *(u32x4*)(C + (size_t)(m0 + r) * ldc + n) = v;
  }
}

constexpr int AQ_LD = 544;
constexpr int VT_LD = 2048 + 8;
__global__ __launch_bounds__(512) void attnA_kernel(const u16* __restrict__ qkv, const u16* __restrict__ kvx, u16* __restrict__ o) {
  __shared__ __attribute__((aligned(16))) u16 Ks[2048 * 16];
  __shared__ __attribute__((aligned(16))) u16 Vt[17 * VT_LD];
  const int tid = threadIdx.x, lane = tid & 63, w = tid >> 6;
  for (int c = tid; c < 2048 * 2; c += 512) *(u32x4*)(Ks + c * 8) = *(const u32x4*)(kvx + c * 8);
  for (int c = tid; c < 16 * 256; c += 512) { const int d = c >> 8, k8 = c & 255; *(u32x4*)(Vt + d * VT_LD + k8 * 8) = *(const u32x4*)(kvx + 2048 * 16 + d * 2048 + k8 * 8); }
  for (int c = tid; c < 256; c += 512) *(u32x4*)(Vt + 16 * VT_LD + c * 8) = u32x4{0x3F803F80u, 0x3F803F80u, 0x3F803F80u, 0x3F803F80u};
  __syncthreads();
  const int tok = blockIdx.x * 8 + w;
  const int c32 = lane & 31, hh = lane >> 5;
  const bf16x8 qb = *(const bf16x8*)(qkv + (size_t)tok * AQ_LD + c32 * 16 + hh * 8);
  const float cs = 0.25f * 1.4426950408889634f;
  float m_run = -INFINITY;
  f32x16 oacc;
#pragma unroll
  for (int i = 0; i < 16; ++i) oacc[i] = 0.f;
  const f32x16 zero16 = oacc;
  const u16* kp = Ks + c32 * 16 + hh * 8;
  const u16* vp = Vt + ((lane & 31) < 16 ? (lane & 15) : 16) * VT_LD + hh * 8;
  auto tile = [&](f32x16& x, int t) {
    float mt = max3f(x[0], x[1], x[2]);
#pragma unroll
    for (int i = 3; i < 15; i += 2) mt = max3f(mt, x[i], x[i + 1]);
    mt = max3f(mt, x[15], x[15]);
    mt = xmax32(mt);
    const float m_new = max3f(m_run, mt, mt);
    const float alpha = __builtin_amdgcn_exp2f((m_run - m_new) * cs);
    const float mc = m_new * cs;
    u32x4 pk0, pk1;
#pragma unroll
    for (int i = 0; i < 16; i += 2) {
      const unsigned pk = cvtpk(__builtin_amdgcn_exp2f(__builtin_fmaf(x[i], cs, -mc)), __builtin_amdgcn_exp2f(__builtin_fmaf(x[i + 1], cs, -mc)));
      if (i < 8) pk0[i >> 1] = pk; else pk1[(i - 8) >> 1] = pk;
    }
    m_run = m_new;
#pragma unroll
    for (int i = 0; i < 9; ++i) oacc[i] *= alpha;
    const bf16x8 va0 = *(const bf16x8*)(vp + t * 32), va1 = *(const bf16x8*)(vp + t * 32 + 16);
    oacc = __builtin_amdgcn_mfma_f32_32x32x16_bf16(va0, __builtin_bit_cast(bf16x8, pk0), oacc, 0, 0, 0);
    oacc = __builtin_amdgcn_mfma_f32_32x32x16_bf16(va1, __builtin_bit_cast(bf16x8, pk1), oacc, 0, 0, 0);
  };
  f32x16 xa = __builtin_amdgcn_mfma_f32_32x32x16_bf16(*(const bf16x8*)(kp), qb, zero16, 0, 0, 0), xb;
  for (int t = 0; t < 64; t += 2) {
    xb = __builtin_amdgcn_mfma_f32_32x32x16_bf16(*(const bf16x8*)(kp + (t + 1) * 32 * 16), qb, zero16, 0, 0, 0);
    tile(xa, t);
    if (t + 2 < 64) xa = __builtin_amdgcn_mfma_f32_32x32x16_bf16(*(const bf16x8*)(kp + (t + 2) * 32 * 16), qb, zero16, 0, 0, 0);
    tile(xb, t + 1);
  }
  const float inv = 1.0f / oacc[8];
  u32x2 o0, o1;
  o0[0] = pack2(oacc[0] * inv, oacc[1] * inv); o0[1] = pack2(oacc[2] * inv, oacc[3] * inv);
  o1[0] = pack2(oacc[4] * inv, oacc[5] * inv); o1[1] = pack2(oacc[6] * inv, oacc[7] * inv);
  u16* op = o + (size_t)tok * 512 + c32 * 16;
  *(u32x2*)(op + 4 * hh) = o0;
  *(u32x2*)(op + 8 + 4 * hh) = o1;
}

DI float red16_max(float v) { v = fmaxf(v, __shfl_xor(v, 1)); v = fmaxf(v, __shfl_xor(v, 2)); v = fmaxf(v, __shfl_xor(v, 4)); v = fmaxf(v, __shfl_xor(v, 8)); return v; }
DI float red16_sum(float v) { v += __shfl_xor(v, 1); v += __shfl_xor(v, 2); v += __shfl_xor(v, 4); v += __shfl_xor(v, 8); return v; }

constexpr float BSCALE = 0.08838834764831845f;
constexpr float NEGBIG = -1000000000.0f;

constexpr unsigned SENT = 0xFFFFFFFFu;
#define AUX_SC1 16
constexpr int KS_LD = 136, VT_LDB = 40, OS_LD = 72;
constexpr int NCH = 64;
constexpr int L_WV = 0, L_WV_BYTES = 4 * 16 * 1024;
constexpr int L_WO = L_WV + L_WV_BYTES, L_WO_BYTES = 32 * 2 * 1024;
constexpr int L_VT = L_WO + L_WO_BYTES, L_VT_BYTES = 64 * VT_LDB * 2;
constexpr int L_K = L_VT + L_VT_BYTES, L_K_BYTES = 40 * KS_LD * 2;
constexpr int L_Q = L_K + L_K_BYTES, L_Q_BYTES = 8 * KS_LD * 2;
constexpr int L_MX = L_Q + L_Q_BYTES, L_MX_BYTES = 8192;
constexpr int L_O = L_MX;
constexpr int L_RS = L_MX + L_MX_BYTES;
constexpr int L_TOTAL = L_RS + 2048;
constexpr int MEMX_SLOT_DW = 64 * 32;
constexpr int INBOX_DW = 64 * 64 * 64;
constexpr unsigned SPIN_LIMIT = 1u << 15;
#define CH_DELAY1 0
#define CH_DELAY2 4

#define STAMP(i)
DI bool valid4(u32x4 v) { return v[0] != SENT && v[1] != SENT && v[2] != SENT && v[3] != SENT; }
DI unsigned canon(unsigned x) { return x == SENT ? 0x7FC07FC0u : x; }

#define LBAR() asm volatile("s_waitcnt lgkmcnt(0)\n\ts_barrier" ::: "memory")
constexpr int CT = 512;

constexpr int CNT_KV = 0, CNT_Q = 16, CNT_SEG = 32, CNT_O = 96, CNT_LINES = 112;
struct BArgs {
  const u16* xn; const u16* Wqkv_t; const u16* Wo_t; const u16* mem0;
  u16* qkvx; u16* vtx; u16* kmall; u16* vmT; u16* ox; float* part;
  unsigned* memx; unsigned* inbox; unsigned* tmo; unsigned* cnt;
};
DI void cnt_add(unsigned* c) { __hip_atomic_fetch_add(c, 1u, __ATOMIC_RELAXED, __HIP_MEMORY_SCOPE_AGENT); }
DI void cnt_wait(__amdgpu_buffer_rsrc_t rc, int line, unsigned n, __amdgpu_buffer_rsrc_t rtm) {
  for (unsigned spins = 0;;) {
    const unsigned v = __builtin_amdgcn_readfirstlane(__builtin_amdgcn_raw_buffer_load_b32(rc, line * 128, 0, AUX_SC1));
    if (v + 1u >= n) return;
    if ((++spins & 15u) == 0u) {
      const unsigned t0 = __builtin_amdgcn_readfirstlane(__builtin_amdgcn_raw_buffer_load_b32(rtm, 0, 0, AUX_SC1));
      if (spins > (SPIN_LIMIT << 2) || t0 == 1u) { __builtin_amdgcn_raw_buffer_store_b32(1u, rtm, 0, 0, AUX_SC1); return; }
    }
    if (spins < 4) __builtin_amdgcn_s_sleep(8); else __builtin_amdgcn_s_sleep(48);
  }
}
#define VMDRAIN() asm volatile("s_waitcnt vmcnt(0)" ::: "memory")

template <int BN, int MODE>
DI void helper_gemm_tile(const BArgs& p, char* smem, int m0, int n0, int kz, int tid) {
  constexpr int BM = 128, LS = 72;
  constexpr int WGN = BN / 32;
  constexpr int WGM = 8 / WGN, WM = BM / WGM;
  constexpr int MT = WM / 16, NT = 2;
  constexpr int K = MODE == 0 ? 512 : 1024;
  constexpr int CA = BM * 8 / CT, CB = (BN * 8 + CT - 1) / CT;
  u16* As = (u16*)smem; u16* Bs = As + BM * LS;
  const int lane = tid & 63, w = tid >> 6, wm = w / WGN, wn = w % WGN, fr = lane & 15, fq = lane >> 4;
  const u16* __restrict__ Bt = (MODE == 0 ? p.Wqkv_t : p.Wo_t) + (MODE == 1 ? kz * K : 0);
  constexpr int ldb = MODE == 0 ? 512 : BHD;
  const __amdgpu_buffer_rsrc_t rox = __builtin_amdgcn_make_buffer_rsrc((void*)p.ox, 0, S_ * BHD * 2, 0x00020000);
  f32x4 acc[MT][NT];
#pragma unroll
  for (int i = 0; i < MT; ++i)
#pragma unroll
    for (int j = 0; j < NT; ++j) acc[i][j] = f32x4{0.f, 0.f, 0.f, 0.f};
  u32x4 ra[CA], rb[CB];
  auto gload = [&](int k0) {
#pragma unroll
    for (int i = 0; i < CA; ++i) { const int c = tid + i * CT, r = c >> 3, c8 = c & 7;
      if (MODE == 0) ra[i] = *(const u32x4*)(p.xn + (size_t)(m0 + r) * 512 + k0 + c8 * 8);
      else ra[i] = __builtin_amdgcn_raw_buffer_load_b128(rox, ((m0 + r) * BHD + kz * K + k0 + c8 * 8) * 2, 0, AUX_SC1); }
#pragma unroll
    for (int i = 0; i < CB; ++i) { const int c = tid + i * CT, r = c >> 3, c8 = c & 7;
      if (c < BN * 8) rb[i] = *(const u32x4*)(Bt + (size_t)(n0 + r) * ldb + k0 + c8 * 8); }
  };
  gload(0);
  for (int k0 = 0; k0 < K; k0 += 64) {
    __syncthreads();
#pragma unroll
    for (int i = 0; i < CA; ++i) { const int c = tid + i * CT, r = c >> 3, c8 = c & 7; *(u32x4*)(As + r * LS + c8 * 8) = ra[i]; }
#pragma unroll
    for (int i = 0; i < CB; ++i) { const int c = tid + i * CT, r = c >> 3, c8 = c & 7; if (c < BN * 8) *(u32x4*)(Bs + r * LS + c8 * 8) = rb[i]; }
    __syncthreads();
    if (k0 + 64 < K) gload(k0 + 64);
#pragma unroll
    for (int kk = 0; kk < 2; ++kk) {
      bf16x8 af[MT], bfr[NT];
#pragma unroll
      for (int i = 0; i < MT; ++i) af[i] = *(const bf16x8*)(As + (wm * WM + i * 16 + fr) * LS + kk * 32 + fq * 8);
#pragma unroll
      for (int j = 0; j < NT; ++j) bfr[j] = *(const bf16x8*)(Bs + (wn * 32 + j * 16 + fr) * LS + kk * 32 + fq * 8);
#pragma unroll
      for (int i = 0; i < MT; ++i)
#pragma unroll
        for (int j = 0; j < NT; ++j) acc[i][j] = __builtin_amdgcn_mfma_f32_16x16x32_bf16(af[i], bfr[j], acc[i][j], 0, 0, 0);
    }
  }
  __syncthreads();
  if (MODE == 0) {
    if (n0 >= 2 * BHD) {
      const __amdgpu_buffer_rsrc_t rv = __builtin_amdgcn_make_buffer_rsrc((void*)p.vtx, 0, S_ * BHD * 2, 0x00020000);
#pragma unroll
      for (int i = 0; i < MT; ++i)
#pragma unroll
        for (int j = 0; j < NT; ++j) {
          const int n = n0 + wn * 32 + j * 16 + fr, m = m0 + wm * WM + i * 16 + fq * 4;
          u32x2 pk; pk[0] = pack2(acc[i][j][0], acc[i][j][1]); pk[1] = pack2(acc[i][j][2], acc[i][j][3]);
          __builtin_amdgcn_raw_buffer_store_b64(pk, rv, ((((m >> 5) * 32 + ((n - 2 * BHD) >> 7)) * 128 + (n & 127)) * 32 + (m & 31)) * 2, 0, AUX_SC1);
        }
    } else {
      constexpr int CS = BN + 8;
      u16* Cs = (u16*)smem;
#pragma unroll
      for (int i = 0; i < MT; ++i)
#pragma unroll
        for (int j = 0; j < NT; ++j)
#pragma unroll
          for (int r = 0; r < 4; ++r) Cs[(wm * WM + i * 16 + fq * 4 + r) * CS + wn * 32 + j * 16 + fr] = f2bf(acc[i][j][r]);
      __syncthreads();
      const __amdgpu_buffer_rsrc_t rq = __builtin_amdgcn_make_buffer_rsrc((void*)p.qkvx, 0, S_ * BN3 * 2, 0x00020000);
      for (int c = tid; c < BM * BN / 8; c += CT) { const int r = c / (BN / 8), c8 = c % (BN / 8);
        __builtin_amdgcn_raw_buffer_store_b128(*(const u32x4*)(Cs + r * CS + c8 * 8), rq, ((m0 + r) * BN3 + n0 + c8 * 8) * 2, 0, AUX_SC1); }
    }
  } else {
    constexpr int FS = BN + 4;
    float* Fs = (float*)smem;
#pragma unroll
    for (int i = 0; i < MT; ++i)
#pragma unroll
      for (int j = 0; j < NT; ++j)
#pragma unroll
        for (int r = 0; r < 4; ++r) Fs[(wm * WM + i * 16 + fq * 4 + r) * FS + wn * 32 + j * 16 + fr] = acc[i][j][r];
    __syncthreads();
    float* Pz = p.part + (size_t)kz * S_ * D_;
    for (int c = tid; c < BM * BN / 4; c += CT) { const int r = c / (BN / 4), c4 = c % (BN / 4);
      *(f32x4*)(Pz + (size_t)(m0 + r) * D_ + n0 + c4 * 4) = *(const f32x4*)(Fs + r * FS + c4 * 4); }
  }
}

DI void attn_task(const BArgs& p, int seg, int h, int lane) {
  const int fr = lane & 15, g = lane >> 4;
  const __amdgpu_buffer_rsrc_t rq = __builtin_amdgcn_make_buffer_rsrc((void*)p.qkvx, 0, S_ * BN3 * 2, 0x00020000);
  const __amdgpu_buffer_rsrc_t rk = __builtin_amdgcn_make_buffer_rsrc((void*)p.kmall, 0, NSEG * 8 * BHD * 2, 0x00020000);
  const __amdgpu_buffer_rsrc_t rvx = __builtin_amdgcn_make_buffer_rsrc((void*)p.vtx, 0, S_ * BHD * 2, 0x00020000);
  const __amdgpu_buffer_rsrc_t rvm = __builtin_amdgcn_make_buffer_rsrc((void*)p.vmT, 0, NSEG * 8 * BHD * 2, 0x00020000);
  const __amdgpu_buffer_rsrc_t rox = __builtin_amdgcn_make_buffer_rsrc((void*)p.ox, 0, S_ * BHD * 2, 0x00020000);
  bf16x8 qf[2][4];
#pragma unroll
  for (int ct = 0; ct < 2; ++ct)
#pragma unroll
    for (int ks = 0; ks < 4; ++ks)
      qf[ct][ks] = __builtin_bit_cast(bf16x8, __builtin_amdgcn_raw_buffer_load_b128(rq, ((seg * 32 + 16 * ct + fr) * BN3 + h * 128 + 8 * g + 32 * ks) * 2, 0, AUX_SC1));
  f32x4 sc[2][3];
#pragma unroll
  for (int t = 0; t < 3; ++t) {
    const int key = 16 * t + fr;
#pragma unroll
    for (int ct = 0; ct < 2; ++ct) sc[ct][t] = f32x4{0.f, 0.f, 0.f, 0.f};
#pragma unroll
    for (int ks = 0; ks < 4; ++ks) {
      u32x4 kv;
      if (key < 8) kv = __builtin_amdgcn_raw_buffer_load_b128(rk, ((seg * 8 + key) * BHD + h * 128 + 8 * g + 32 * ks) * 2, 0, AUX_SC1);
      else kv = __builtin_amdgcn_raw_buffer_load_b128(rq, ((seg * 32 + (key < 40 ? key - 8 : 0)) * BN3 + BHD + h * 128 + 8 * g + 32 * ks) * 2, 0, AUX_SC1);
      const bf16x8 kf = __builtin_bit_cast(bf16x8, kv);
#pragma unroll
      for (int ct = 0; ct < 2; ++ct) sc[ct][t] = __builtin_amdgcn_mfma_f32_16x16x32_bf16(kf, qf[ct][ks], sc[ct][t], 0, 0, 0);
    }
  }
  const float negbig = rbf(NEGBIG);
  bf16x8 pf[2][2];
#pragma unroll
  for (int ct = 0; ct < 2; ++ct) {
    const int row = BM_ + 16 * ct + fr;
    float pv[3][4]; float mx = -INFINITY;
#pragma unroll
    for (int t = 0; t < 3; ++t)
#pragma unroll
      for (int r = 0; r < 4; r += 2) {
        const unsigned c1 = cvtpk(sc[ct][t][r], sc[ct][t][r + 1]);
        const unsigned c2 = cvtpk(__uint_as_float(c1 << 16) * BSCALE, __uint_as_float(c1 & 0xffff0000u) * BSCALE);
        float v0 = __uint_as_float(c2 << 16), v1 = __uint_as_float(c2 & 0xffff0000u);
        const int key = 16 * t + 4 * g + r;
        if (key > row) v0 = negbig;
        if (key + 1 > row) v1 = negbig;
        if (t == 2 && g >= 2) { v0 = -INFINITY; v1 = -INFINITY; }
        pv[t][r] = v0; pv[t][r + 1] = v1; mx = max3f(mx, v0, v1);
      }
    mx = xmax16(mx); mx = xmax32(mx);
    float sum = 0.f;
#pragma unroll
    for (int t = 0; t < 3; ++t)
#pragma unroll
      for (int r = 0; r < 4; ++r) { pv[t][r] = __expf(pv[t][r] - mx); sum += pv[t][r]; }
    sum = xsum16(sum); sum = xsum32(sum);
    const float inv = 1.0f / sum;
    u32x4 p0, p1;
    p0[0] = cvtpk(pv[0][0] * inv, pv[0][1] * inv); p0[1] = cvtpk(pv[0][2] * inv, pv[0][3] * inv);
    p0[2] = cvtpk(pv[1][0] * inv, pv[1][1] * inv); p0[3] = cvtpk(pv[1][2] * inv, pv[1][3] * inv);
    p1[0] = cvtpk(pv[2][0] * inv, pv[2][1] * inv); p1[1] = cvtpk(pv[2][2] * inv, pv[2][3] * inv);
    p1[2] = 0u; p1[3] = 0u;
    pf[ct][0] = __builtin_bit_cast(bf16x8, p0); pf[ct][1] = __builtin_bit_cast(bf16x8, p1);
  }
  const int vb = (seg * 32 + h) * 128;
#pragma unroll
  for (int d = 0; d < 8; ++d) {
    const int dim = 16 * d + fr;
    const int vxo = (vb + dim) * 32 * 2;
    const u32x2 lo0 = g < 2 ? __builtin_amdgcn_raw_buffer_load_b64(rvm, ((vb + dim) * 8 + 4 * g) * 2, 0, AUX_SC1)
                            : __builtin_amdgcn_raw_buffer_load_b64(rvx, vxo + 8 * (g - 2), 0, AUX_SC1);
    const u32x2 hi0 = __builtin_amdgcn_raw_buffer_load_b64(rvx, vxo + (8 + 4 * g) * 2, 0, AUX_SC1);
    const u32x2 lo1 = __builtin_amdgcn_raw_buffer_load_b64(rvx, vxo + (24 + 4 * (g & 1)) * 2, 0, AUX_SC1);
    const u32x4 va0 = {lo0[0], lo0[1], hi0[0], hi0[1]}, va1 = {lo1[0], lo1[1], 0u, 0u};
#pragma unroll
    for (int ct = 0; ct < 2; ++ct) {
      f32x4 oa = {0.f, 0.f, 0.f, 0.f};
      oa = __builtin_amdgcn_mfma_f32_16x16x32_bf16(__builtin_bit_cast(bf16x8, va0), pf[ct][0], oa, 0, 0, 0);
      oa = __builtin_amdgcn_mfma_f32_16x16x32_bf16(__builtin_bit_cast(bf16x8, va1), pf[ct][1], oa, 0, 0, 0);
      u32x2 pk; pk[0] = cvtpk(oa[0], oa[1]); pk[1] = cvtpk(oa[2], oa[3]);
      __builtin_amdgcn_raw_buffer_store_b64(pk, rox, ((seg * 32 + 16 * ct + fr) * BHD + h * 128 + 16 * d + 4 * g) * 2, 0, AUX_SC1);
    }
  }
}

constexpr int NHELP = 192;
DI void helper_main(const BArgs& p, char* smem, int hid, int tid) {
  const int lane = tid & 63, w = __builtin_amdgcn_readfirstlane(tid >> 6);
  const __amdgpu_buffer_rsrc_t rc = __builtin_amdgcn_make_buffer_rsrc((void*)p.cnt, 0, CNT_LINES * 128, 0x00020000);
  const __amdgpu_buffer_rsrc_t rtm = __builtin_amdgcn_make_buffer_rsrc((void*)p.tmo, 0, 64, 0x00020000);
  for (int t = hid; t < 1536; t += NHELP) {
    int rb, cb;
    if (t < 1024) { rb = t >> 6; cb = 32 + (t & 63); } else { rb = (t - 1024) >> 5; cb = (t - 1024) & 31; }
    helper_gemm_tile<128, 0>(p, smem, rb * 128, cb * 128, 0, tid);
    VMDRAIN();
    __syncthreads();
    if (tid == 0) cnt_add(p.cnt + (t < 1024 ? CNT_KV + rb : CNT_Q + rb) * 32);
  }
  for (int t8 = hid; t8 < 256; t8 += NHELP) {
    const int seg = t8 >> 2, h = (t8 & 3) * 8 + w, rb = seg >> 2;
    if (w == 0) { cnt_wait(rc, CNT_KV + rb, 64, rtm); cnt_wait(rc, CNT_Q + rb, 32, rtm); cnt_wait(rc, CNT_SEG + seg, NCH, rtm); }
    __syncthreads();
    attn_task(p, seg, h, lane);
    VMDRAIN();
    if (lane == 0) cnt_add(p.cnt + (CNT_O + rb) * 32);
  }
  for (int it = hid; it < 512; it += NHELP) {
    const int rb = it >> 5, nb = (it >> 2) & 7, kz = it & 3;
    if (w == 0) cnt_wait(rc, CNT_O + rb, 128, rtm);
    __syncthreads();
    helper_gemm_tile<64, 1>(p, smem, rb * 128, nb * 64, kz, tid);
  }
}

__global__ __launch_bounds__(CT, 2) void blayer_kernel(BArgs p) {
  __shared__ __attribute__((aligned(16))) char smem[L_TOTAL];
  if (blockIdx.x >= NCH) { helper_main(p, smem, blockIdx.x - NCH, threadIdx.x); return; }
  const u16* __restrict__ Wqkv_t = p.Wqkv_t; const u16* __restrict__ Wo_t = p.Wo_t; const u16* __restrict__ mem0 = p.mem0;
  const u16* qkvx = p.qkvx; const u16* vtx = p.vtx; u16* kmall = p.kmall; u16* vmTall = p.vmT;
  unsigned* memx = p.memx; unsigned* inbox = p.inbox; unsigned* tmo = p.tmo;
  const int b = blockIdx.x, h = b >> 1, a = b & 1;
  const int tid = threadIdx.x, lane = tid & 63, w = __builtin_amdgcn_readfirstlane(tid >> 6), fr = lane & 15, g = lane >> 4;
  u16* VT = (u16*)(smem + L_VT); u16* KALL = (u16*)(smem + L_K); u16* QS = (u16*)(smem + L_Q); u16* OS = (u16*)(smem + L_O);
  const __amdgpu_buffer_rsrc_t rmx = __builtin_amdgcn_make_buffer_rsrc((void*)memx, 0, (NSEG + 1) * MEMX_SLOT_DW * 4, 0x00020000);
  const __amdgpu_buffer_rsrc_t rin = __builtin_amdgcn_make_buffer_rsrc((void*)inbox, 0, 2 * INBOX_DW * 4, 0x00020000);
  const __amdgpu_buffer_rsrc_t rtm = __builtin_amdgcn_make_buffer_rsrc((void*)tmo, 0, 64, 0x00020000);
  const __amdgpu_buffer_rsrc_t rqk = __builtin_amdgcn_make_buffer_rsrc((void*)qkvx, 0, S_ * BN3 * 2, 0x00020000);
  const __amdgpu_buffer_rsrc_t rvx = __builtin_amdgcn_make_buffer_rsrc((void*)vtx, 0, S_ * BHD * 2, 0x00020000);
  const __amdgpu_buffer_rsrc_t rkm = __builtin_amdgcn_make_buffer_rsrc((void*)kmall, 0, NSEG * 8 * BHD * 2, 0x00020000);
  const __amdgpu_buffer_rsrc_t rvm = __builtin_amdgcn_make_buffer_rsrc((void*)vmTall, 0, NSEG * 8 * BHD * 2, 0x00020000);
  const __amdgpu_buffer_rsrc_t rcn = __builtin_amdgcn_make_buffer_rsrc((void*)p.cnt, 0, CNT_LINES * 128, 0x00020000);

  bf16x8 wf[2][16];
#pragma unroll
  for (int t = 0; t < 2; ++t) {
    const u16* src = Wqkv_t + (size_t)(t * BHD + h * 128 + 16 * w + fr) * 512 + g * 8;
#pragma unroll
    for (int ks = 0; ks < 16; ++ks) wf[t][ks] = *(const bf16x8*)(src + ks * 32);
  }
  {
#pragma unroll
    for (int i = 0; i < 8; ++i) { const int f = w * 8 + i, j = f >> 4, ks = f & 15;
      *(bf16x8*)(smem + L_WV + (f * 64 + lane) * 16) = *(const bf16x8*)(Wqkv_t + (size_t)(2 * BHD + h * 128 + 64 * a + 16 * j + fr) * 512 + ks * 32 + g * 8); }
#pragma unroll
    for (int i = 0; i < 8; ++i) { const int f = w * 8 + i, nt = f >> 1, ks2 = f & 1;
      *(bf16x8*)(smem + L_WO + (f * 64 + lane) * 16) = *(const bf16x8*)(Wo_t + (size_t)(16 * nt + fr) * BHD + h * 128 + 64 * a + ks2 * 32 + g * 8); }
  }
  for (int c = tid; c < L_VT_BYTES / 16; c += CT) *(u32x4*)(smem + L_VT + c * 16) = u32x4{0u, 0u, 0u, 0u};
  LBAR();

  u32x4 kst, vst;
  const int ktok = tid >> 4, kd8 = tid & 15;
  const int vdim = tid >> 2, vc = tid & 3;
  const int k_voff = (ktok * BN3 + BHD + h * 128 + kd8 * 8) * 2;
  const int v_voff = ((h * 128 + 64 * a + vdim) * 32 + vc * 8) * 2;
  auto kv_load = [&](int s) {
    if ((s & 3) == 0) cnt_wait(rcn, CNT_KV + (s >> 2), 64, rtm);
    kst = __builtin_amdgcn_raw_buffer_load_b128(rqk, k_voff, s * 32 * BN3 * 2, AUX_SC1);
    if (tid < 256) vst = __builtin_amdgcn_raw_buffer_load_b128(rvx, v_voff, s * 32 * 128 * 32 * 2, AUX_SC1);
  };
  auto kv_store = [&]() {
    *(u32x4*)(KALL + (8 + ktok) * KS_LD + kd8 * 8) = kst;
    if (tid < 256) *(u32x4*)(VT + vdim * VT_LDB + 8 + vc * 8) = vst;
  };
  const int km_voff = (fr * BHD + h * 128 + 16 * w + 4 * g) * 2;
  const int vm_voff = ((h * 128 + 64 * a + 16 * w + fr) * 8 + 4 * g) * 2;
  const int ib_voff = (((g >> 1) * 64 + b) * 64 + fr * 8 + 4 * (g & 1)) * 4;
  const int ip_voff = (b * 64 * 64 + tid * 4) * 4;
  const int mp_voff = tid * 16;
  kv_load(0);
  kv_store();
  { const int r = tid >> 6, k = (tid & 63) * 8;
    *(u32x4*)(smem + L_MX + (k >> 3) * 128 + r * 16) = *(const u32x4*)(mem0 + r * 512 + k); }

  const char* mxl = smem + L_MX + g * 128 + (fr & 7) * 16;
  for (int s = 0; s < NSEG; ++s) {
    LBAR();
    if (s + 1 < NSEG) kv_load(s + 1);
    f32x4 acc[2];
#pragma unroll
    for (int t = 0; t < 2; ++t) acc[t] = f32x4{0.f, 0.f, 0.f, 0.f};
    {
      bf16x8 mq[4];
#pragma unroll
      for (int i = 0; i < 4; ++i) mq[i] = *(const bf16x8*)(mxl + i * 512);
#pragma unroll
      for (int ks = 0; ks < 16; ++ks) {
        const bf16x8 mf = mq[ks & 3];
        if (ks + 4 < 16) mq[ks & 3] = *(const bf16x8*)(mxl + (ks + 4) * 512);
        acc[0] = __builtin_amdgcn_mfma_f32_16x16x32_bf16(wf[0][ks], mf, acc[0], 0, 0, 0);
        acc[1] = __builtin_amdgcn_mfma_f32_16x16x32_bf16(wf[1][ks], mf, acc[1], 0, 0, 0);
      }
    }
    STAMP(0);
    {
      const int n0 = 16 * w + 4 * g;
      u32x2 pq, pk2;
      pq[0] = pack2(acc[0][0], acc[0][1]); pq[1] = pack2(acc[0][2], acc[0][3]);
      pk2[0] = pack2(acc[1][0], acc[1][1]); pk2[1] = pack2(acc[1][2], acc[1][3]);
      if (fr < 8) { *(u32x2*)(QS + fr * KS_LD + n0) = pq; *(u32x2*)(KALL + fr * KS_LD + n0) = pk2;
                    if (a == 0) __builtin_amdgcn_raw_buffer_store_b64(pk2, rkm, km_voff, s * 8 * BHD * 2, AUX_SC1); }
    }
    LBAR();
    STAMP(1);
    if (w >= 4) __builtin_amdgcn_s_setprio(3);
    bf16x8 pf[2];
    if (w < 4) {
      const char* wvb = smem + L_WV + (w * 16 * 64 + lane) * 16;
      f32x4 av = {0.f, 0.f, 0.f, 0.f};
      bf16x8 mfa = *(const bf16x8*)(mxl), mfb = *(const bf16x8*)(mxl + 512);
      bf16x8 wva = *(const bf16x8*)(wvb), wvb2 = *(const bf16x8*)(wvb + 1024);
#pragma unroll
      for (int ks = 0; ks < 16; ++ks) {
        const bf16x8 mf = (ks & 1) ? mfb : mfa, wv = (ks & 1) ? wvb2 : wva;
        if (ks + 2 < 16) {
          if (ks & 1) { mfb = *(const bf16x8*)(mxl + (ks + 2) * 512); wvb2 = *(const bf16x8*)(wvb + (ks + 2) * 1024); }
          else { mfa = *(const bf16x8*)(mxl + (ks + 2) * 512); wva = *(const bf16x8*)(wvb + (ks + 2) * 1024); }
        }
        av = __builtin_amdgcn_mfma_f32_16x16x32_bf16(mf, wv, av, 0, 0, 0);
      }
      u32x2 pv2; pv2[0] = pack2(av[0], av[1]); pv2[1] = pack2(av[2], av[3]);
      if (g < 2) { *(u32x2*)(VT + (16 * w + fr) * VT_LDB + 4 * g) = pv2; __builtin_amdgcn_raw_buffer_store_b64(pv2, rvm, vm_voff, s * 32 * 128 * 8 * 2, AUX_SC1); }
    } else {
      bf16x8 qf[4];
#pragma unroll
      for (int ks = 0; ks < 4; ++ks) qf[ks] = *(const bf16x8*)(QS + (fr & 7) * KS_LD + 32 * ks + 8 * g);
      f32x4 sc[3];
      {
        const u16* kb = KALL + fr * KS_LD + 8 * g;
        bf16x8 kf[3][4];
#pragma unroll
        for (int t = 0; t < 3; ++t)
#pragma unroll
          for (int ks = 0; ks < 4; ++ks) kf[t][ks] = *(const bf16x8*)(kb + 16 * t * KS_LD + 32 * ks);
#pragma unroll
        for (int t = 0; t < 3; ++t) sc[t] = f32x4{0.f, 0.f, 0.f, 0.f};
#pragma unroll
        for (int ks = 0; ks < 4; ++ks)
#pragma unroll
          for (int t = 0; t < 3; ++t) sc[t] = __builtin_amdgcn_mfma_f32_16x16x32_bf16(kf[t][ks], qf[ks], sc[t], 0, 0, 0);
      }
      float pv[3][4]; float mx = -INFINITY;
#pragma unroll
      for (int t = 0; t < 3; ++t)
#pragma unroll
        for (int r = 0; r < 4; r += 2) {
          const unsigned c1 = pack2(sc[t][r], sc[t][r + 1]);
          const unsigned c2 = pack2(__uint_as_float(c1 << 16) * BSCALE, __uint_as_float(c1 & 0xffff0000u) * BSCALE);
          float v0 = __uint_as_float(c2 << 16), v1 = __uint_as_float(c2 & 0xffff0000u);
          if (t == 2 && g >= 2) { v0 = -INFINITY; v1 = -INFINITY; }
          pv[t][r] = v0; pv[t][r + 1] = v1; mx = max3f(mx, v0, v1);
        }
      mx = xmax16(mx); mx = xmax32(mx);
      float sum = 0.f;
#pragma unroll
      for (int t = 0; t < 3; ++t)
#pragma unroll
        for (int r = 0; r < 4; ++r) { pv[t][r] = __expf(pv[t][r] - mx); sum += pv[t][r]; }
      sum = xsum16(sum); sum = xsum32(sum);
      const float inv = 1.0f / sum;
      u32x4 p0, p1;
      p0[0] = pack2(pv[0][0] * inv, pv[0][1] * inv); p0[1] = pack2(pv[0][2] * inv, pv[0][3] * inv);
      p0[2] = pack2(pv[1][0] * inv, pv[1][1] * inv); p0[3] = pack2(pv[1][2] * inv, pv[1][3] * inv);
      p1[0] = pack2(pv[2][0] * inv, pv[2][1] * inv); p1[1] = pack2(pv[2][2] * inv, pv[2][3] * inv);
      p1[2] = 0u; p1[3] = 0u;
      pf[0] = __builtin_bit_cast(bf16x8, p0); pf[1] = __builtin_bit_cast(bf16x8, p1);
    }
    LBAR();
    if (w >= 4) {
      const u16* vrow = VT + (16 * (w & 3) + fr) * VT_LDB + 4 * g;
      const u32x2 v00 = *(const u32x2*)(vrow), v01 = *(const u32x2*)(vrow + 16), v10 = *(const u32x2*)(vrow + 32), v11 = *(const u32x2*)(vrow + 48);
      const u32x4 va0 = {v00[0], v00[1], v01[0], v01[1]}, va1 = {v10[0], v10[1], v11[0], v11[1]};
      f32x4 oa = {0.f, 0.f, 0.f, 0.f};
      oa = __builtin_amdgcn_mfma_f32_16x16x32_bf16(__builtin_bit_cast(bf16x8, va0), pf[0], oa, 0, 0, 0);
      oa = __builtin_amdgcn_mfma_f32_16x16x32_bf16(__builtin_bit_cast(bf16x8, va1), pf[1], oa, 0, 0, 0);
      u32x2 pk; pk[0] = pack2(oa[0], oa[1]); pk[1] = pack2(oa[2], oa[3]);
      if (fr < 8) *(u32x2*)(OS + fr * OS_LD + 16 * (w & 3) + 4 * g) = pk;
    }
    __builtin_amdgcn_s_setprio(0);
    bf16x8 wo[8];
    {
      const char* wob = smem + L_WO + (w * 8 * 64 + lane) * 16;
#pragma unroll
      for (int i = 0; i < 8; ++i) wo[i] = *(const bf16x8*)(wob + i * 1024);
    }
    LBAR();
    STAMP(2);
    if (s + 1 == NSEG) { VMDRAIN(); LBAR(); if (tid == 0) cnt_add(p.cnt + (CNT_SEG + s) * 32); break; }
    const int slot = s & 1;
    {
      bf16x8 of[2];
#pragma unroll
      for (int ks = 0; ks < 2; ++ks) of[ks] = *(const bf16x8*)(OS + (fr & 7) * OS_LD + 32 * ks + 8 * g);
#pragma unroll
      for (int i = 0; i < 4; ++i) {
        f32x4 acc2 = {0.f, 0.f, 0.f, 0.f};
        acc2 = __builtin_amdgcn_mfma_f32_16x16x32_bf16(wo[2 * i], of[0], acc2, 0, 0, 0);
        acc2 = __builtin_amdgcn_mfma_f32_16x16x32_bf16(wo[2 * i + 1], of[1], acc2, 0, 0, 0);
        u32x4 st;
#pragma unroll
        for (int r = 0; r < 4; ++r) { const unsigned ub = __float_as_uint(acc2[r]); st[r] = ub == SENT ? 0x7FC00000u : ub; }
        if (fr < 8) __builtin_amdgcn_raw_buffer_store_b128(st, rin, ib_voff, (slot * INBOX_DW + (4 * w + i) * 2 * 64 * 64) * 4, AUX_SC1);
      }
    }
    STAMP(3);
    kv_store();
    STAMP(4);
#pragma unroll
    for (int i = 0; i < CH_DELAY1; ++i) __builtin_amdgcn_s_sleep(4);
    u32x4 pib[2];
    {
      unsigned spins = 0;
      for (;;) {
        bool ok = true;
#pragma unroll
        for (int i = 0; i < 2; ++i) { pib[i] = __builtin_amdgcn_raw_buffer_load_b128(rin, ip_voff, (slot * INBOX_DW + CT * i * 4) * 4, AUX_SC1); ok &= valid4(pib[i]); }
        if (__all(ok)) break;
        if ((++spins & 63u) == 0u) {
          const unsigned t0 = __builtin_amdgcn_readfirstlane(__builtin_amdgcn_raw_buffer_load_b32(rtm, 0, 0, AUX_SC1));
          if (spins > SPIN_LIMIT || t0 == 1u) { if (lane == 0) __builtin_amdgcn_raw_buffer_store_b32(1u, rtm, 0, 0, AUX_SC1); break; }
        }
        __builtin_amdgcn_s_sleep(2);
      }
    }
    STAMP(5);
    {
      f32x4 ps;
#pragma unroll
      for (int r = 0; r < 4; ++r) ps[r] = xsum32(xsum16(__uint_as_float(pib[0][r]) + __uint_as_float(pib[1][r])));
      if (lane < 16) *(f32x4*)(smem + L_RS + (w * 16 + lane) * 16) = ps;
    }
    LBAR();
    if (tid == 64) cnt_add(p.cnt + (CNT_SEG + s) * 32);
    if (tid < 16) {
      f32x4 t4 = *(const f32x4*)(smem + L_RS + tid * 16);
#pragma unroll
      for (int j = 1; j < 8; ++j) { const f32x4 o4 = *(const f32x4*)(smem + L_RS + (j * 16 + tid) * 16); t4 = t4 + o4; }
      u32x2 pk; pk[0] = canon(pack2(t4[0], t4[1])); pk[1] = canon(pack2(t4[2], t4[3]));
      __builtin_amdgcn_raw_buffer_store_b64(pk, rmx, (b * 32 + tid * 2) * 4, (s + 1) * MEMX_SLOT_DW * 4, AUX_SC1);
    }
    {
      const u32x4 sent = {SENT, SENT, SENT, SENT};
#pragma unroll
      for (int i = 0; i < 2; ++i) __builtin_amdgcn_raw_buffer_store_b128(sent, rin, ip_voff, (slot * INBOX_DW + CT * i * 4) * 4, AUX_SC1);
    }
    STAMP(6);
#pragma unroll
    for (int i = 0; i < CH_DELAY2; ++i) __builtin_amdgcn_s_sleep(4);
    {
      u32x4 m;
      unsigned spins = 0;
      for (;;) {
        m = __builtin_amdgcn_raw_buffer_load_b128(rmx, mp_voff, (s + 1) * MEMX_SLOT_DW * 4, AUX_SC1);
        if (__all(valid4(m))) break;
        if ((++spins & 63u) == 0u) {
          const unsigned t0 = __builtin_amdgcn_readfirstlane(__builtin_amdgcn_raw_buffer_load_b32(rtm, 0, 0, AUX_SC1));
          if (spins > SPIN_LIMIT || t0 == 1u) { if (lane == 0) __builtin_amdgcn_raw_buffer_store_b32(1u, rtm, 0, 0, AUX_SC1); break; }
        }
        __builtin_amdgcn_s_sleep(2);
      }
      *(u32x4*)(smem + L_MX + tid * 16) = m;
    }
    STAMP(7);
  }
}

extern "C" void kernel_launch(void* const* d_in, const int* in_sizes, int n_in,
                              void* d_out, int out_size, void* d_ws, size_t ws_size,
                              hipStream_t stream) {
  const u16* A_x = (const u16*)d_in[0];
  const u16* A_wq = (const u16*)d_in[1];
  const u16* A_wk = (const u16*)d_in[2];
  const u16* A_wv = (const u16*)d_in[3];
  const u16* A_wo = (const u16*)d_in[4];
  const u16* B_mem0 = (const u16*)d_in[6];
  const u16* B_Wq = (const u16*)d_in[7];
  const u16* B_Wk = (const u16*)d_in[8];
  const u16* B_Wv = (const u16*)d_in[9];
  const u16* B_Wo = (const u16*)d_in[10];
  u16* out = (u16*)d_out;

  char* ws = (char*)d_ws; size_t off = 0;
  auto alloc = [&](size_t bytes) { char* p = ws + off; off += (bytes + 255) & ~(size_t)255; return (u16*)p; };
  u16* xn = alloc((size_t)S_ * D_ * 2);
  u16* x1 = alloc((size_t)S_ * D_ * 2);
  u16* x2 = alloc((size_t)S_ * D_ * 2);
  u16* x3 = alloc((size_t)S_ * D_ * 2);
  u16* aqkv = alloc((size_t)S_ * AQ_LD * 2);
  u16* ao = alloc((size_t)S_ * D_ * 2);
  u16* Awqkv_t = alloc((size_t)AQ_LD * D_ * 2);
  u16* Awo_t = alloc((size_t)D_ * D_ * 2);
  u16* BWqkv_t = alloc((size_t)BN3 * D_ * 2);
  u16* BWo_t = alloc((size_t)D_ * BHD * 2);
  u16* qkvx = alloc((size_t)S_ * BN3 * 2);
  u16* ox = alloc((size_t)S_ * BHD * 2);
  u16* vtx = alloc((size_t)S_ * BHD * 2);
  u16* akvx = alloc((size_t)S_ * 32 * 2);
  u16* kmall = alloc((size_t)NSEG * 8 * BHD * 2);
  u16* vmall = alloc((size_t)NSEG * 8 * BHD * 2);
  char* exch0 = ws + off;
  unsigned* tmo = (unsigned*)alloc(256 + 64 * 8 * 8 * 8);
  unsigned* memx = (unsigned*)alloc((size_t)2 * (NSEG + 1) * MEMX_SLOT_DW * 4);
  unsigned* inbox = (unsigned*)alloc((size_t)2 * INBOX_DW * 4);
  unsigned* cnts = (unsigned*)alloc((size_t)2 * CNT_LINES * 128);
  int blayer = 0;

  {
    TrJobs jb;
    const u16* srcs[8] = {A_wq, A_wk, A_wv, A_wo, B_Wq, B_Wk, B_Wv, B_Wo};
    u16* dsts[8] = {Awqkv_t, Awqkv_t + 512 * 512, Awqkv_t + 528 * 512, Awo_t, BWqkv_t, BWqkv_t + (size_t)4096 * 512, BWqkv_t + (size_t)8192 * 512, BWo_t};
    const int Ks[8] = {512, 512, 512, 512, 512, 512, 512, 4096}, Ns[8] = {512, 16, 16, 512, 4096, 4096, 4096, 512};
    int t0 = 0;
    for (int i = 0; i < 8; ++i) { jb.src[i] = srcs[i]; jb.dst[i] = dsts[i]; jb.K[i] = Ks[i]; jb.N[i] = Ns[i]; jb.tile0[i] = t0; t0 += (Ks[i] / 64) * ((Ns[i] + 63) / 64); }
    jb.tile0[8] = t0;
    jb.fill = (unsigned*)exch0; jb.fill_u4 = (int)(((ws + off) - exch0) / 16);
    jb.lnx = A_x; jb.lny = xn;
    prologue_kernel<<<t0 + 64 + 512, 256, 0, stream>>>(jb);
  }

  float* part = (float*)alloc((size_t)4 * S_ * D_ * 4);
  auto layerA = [&](const u16* xin, u16* xout, bool ln_next) {
    gemm_nt<64, 64, 3><<<dim3(9, 32), 256, 0, stream>>>(xn, 512, Awqkv_t, 512, aqkv, AQ_LD, AQ_LD, 512, nullptr, akvx);
    attnA_kernel<<<256, 512, 0, stream>>>(aqkv, akvx, ao);
    gemm_nt<64, 64, 4><<<dim3(8, 32, 1), 256, 0, stream>>>(ao, 512, Awo_t, 512, nullptr, 512, 512, 512, nullptr, (u16*)part);
    finish_ln_kernel<1><<<512, 256, 0, stream>>>(part, xin, xout, ln_next ? xn : nullptr);
  };
  auto layerB = [&](const u16* xin, u16* xout, bool ln_next) {
    BArgs ba;
    ba.xn = xn; ba.Wqkv_t = BWqkv_t; ba.Wo_t = BWo_t; ba.mem0 = B_mem0;
    ba.qkvx = qkvx; ba.vtx = vtx; ba.kmall = kmall; ba.vmT = vmall; ba.ox = ox; ba.part = part;
    ba.memx = memx + (size_t)blayer * (NSEG + 1) * MEMX_SLOT_DW; ba.inbox = inbox; ba.tmo = tmo; ba.cnt = cnts + blayer * CNT_LINES * 32;
    blayer_kernel<<<NCH + NHELP, CT, 0, stream>>>(ba);
    ++blayer;
    finish_ln_kernel<4><<<512, 256, 0, stream>>>(part, xin, xout, ln_next ? xn : nullptr);
  };
  layerA(A_x, x1, true);
  layerB(x1, x2, true);
  layerA(x2, x3, true);
  layerB(x3, out, false);
}
```
